```python
import torch

def _softmax(x, axis=-1):
    return torch.softmax(x, dim=axis)

def _gelu(x):
    return torch.nn.functional.gelu(x, approximate='tanh')

def _silu(x):
    return torch.nn.functional.silu(x)

def _sigmoid(x):
    return torch.sigmoid(x)

def _layernorm(x, axis=-1, eps=1e-05):
    m = x.mean(dim=axis, keepdim=True)
    v = x.var(dim=axis, keepdim=True, unbiased=False)
    return (x - m) / torch.sqrt(v + eps)

def _scatter_add(tgt, idx, src, axis=0):
    return tgt.clone().index_add_(axis, idx, src)

def _segment_sum(src, seg, n):
    out = torch.zeros((n,) + src.shape[1:], dtype=src.dtype)
    return out.index_add_(0, seg, src)

def _pad(x, pw):
    flat = tuple((v for pair in reversed(pw) for v in pair))
    return torch.nn.functional.pad(x, flat)
kernel_name = 'recurrent_scan__ssm_conv_h3__mixer_gmlp__prompt'

def setup_inputs():

    _w = lambda a: (lambda t: t.to(torch.bfloat16) if t.is_floating_point() else t)(torch.as_tensor(a))

    def _A_setup():
        B, S, D = (1, 2048, 448)
        ks = 4
        return {'q': _w((lambda *s: torch.randn(*s, dtype=torch.bfloat16) * 0.05)(B, S, D)), 'k': _w((lambda *s: torch.randn(*s, dtype=torch.bfloat16) * 0.2)(B, S, D)), 'v': _w((lambda *s: torch.randn(*s, dtype=torch.bfloat16) * 0.2)(B, S, D)), 'shift_k': _w((lambda *s: torch.randn(*s, dtype=torch.bfloat16) * 0.2)(D, ks) * 0.5), 'ssm_k': _w((lambda *s: torch.randn(*s, dtype=torch.bfloat16) * 0.2)(D, S) * (1.0 / S ** 0.5)), 'ssm_D': _w((lambda *s: torch.randn(*s, dtype=torch.bfloat16) * 0.2)(D))}
    _A_in = _A_setup()

    def _B_setup():
        B, S, D, F = (1, 2048, 448, 1792)
        Fh = F // 2
        return {'x': _w((lambda *s: torch.randn(*s, dtype=torch.bfloat16) * 0.05)(B, S, D)), 'w_in': _w((lambda *s: torch.randn(*s, dtype=torch.bfloat16) * 0.8)(D, 2 * Fh)), 'w_sp': _w(0.01 * (lambda *s: torch.randn(*s, dtype=torch.bfloat16) * 0.8)(S, S)), 'b_sp': _w(1.0 + 0.01 * (lambda *s: torch.randn(*s, dtype=torch.bfloat16) * 0.1)(S)), 'w_out': _w((lambda *s: torch.randn(*s, dtype=torch.bfloat16) * 0.1)(Fh, D))}
    _B_in = _B_setup()
    return {'A_q': _A_in['q'], 'A_k': _A_in['k'], 'A_v': _A_in['v'], 'A_shift_k': _A_in['shift_k'], 'A_ssm_k': _A_in['ssm_k'], 'A_ssm_D': _A_in['ssm_D'], 'B_x': _B_in['x'], 'B_w_in': _B_in['w_in'], 'B_w_sp': _B_in['w_sp'], 'B_b_sp': _B_in['b_sp'], 'B_w_out': _B_in['w_out']}

def reference(A_q, A_k, A_v, A_shift_k, A_ssm_k, A_ssm_D, B_x, B_w_in, B_w_sp, B_b_sp, B_w_out):

    def _A_ref(q, k, v, shift_k, ssm_k, ssm_D):
        B, S, D = q.shape
        ks = shift_k.shape[-1]
        k_pad = _pad(k, ((0, 0), (ks - 1, 0), (0, 0)))
        k_shift = torch.zeros_like(k)
        for t in range(ks):
            k_shift = k_shift + k_pad[:, t:t + S, :] * shift_k[:, ks - 1 - t][None, None, :]
        kv = k_shift * v
        L = 2 * S
        kv_pad = _pad(kv, ((0, 0), (0, S), (0, 0)))
        kf_pad = _pad(ssm_k, ((0, 0), (0, S)))
        kv_f = (lambda x, axes=(-2, -1): torch.fft.rfft2(x.float(), dim=axes))(kv_pad[:, None, :, :], axes=(1, 2))
        kf_f = (lambda x, axes=(-2, -1): torch.fft.rfft2(x.float(), dim=axes))(kf_pad[:, None, :], axes=(1, 2))
        kf_f = (lambda x, *d: x.permute(*d))(kf_f, 1, 2, 0)
        y_f = kv_f * kf_f[None, :, :, :]
        y = (lambda x, s, axes=(-2, -1): torch.fft.irfft2(x, s=s, dim=axes).to(torch.bfloat16))(y_f, (1, L), axes=(1, 2))[:, 0, :S, :]
        y = y + kv * ssm_D[None, None, :]
        return q * y

    def _B_ref(x, w_in, w_sp, b_sp, w_out):
        B, S, D = x.shape
        F = w_in.shape[-1]
        Fh = F // 2
        z = _gelu(torch.einsum('bsd,df->bsf', x, w_in))
        z1 = z[..., :Fh].clone()
        z2 = _layernorm(z[..., Fh:], axis=-1)
        g = torch.einsum('ts,bsf->btf', w_sp, z2) + b_sp[None, :, None]
        return torch.einsum('bsf,fd->bsd', z1 * g, w_out)
    x = A_q
    xn = _layernorm(x, axis=-1)
    s = _A_ref(xn, A_k, A_v, A_shift_k, A_ssm_k, A_ssm_D)
    g = _silu(_B_ref(xn, B_w_in, B_w_sp, B_b_sp, B_w_out))
    return x + s * g
```

```cpp
#include <hip/hip_runtime.h>
#include <hip/hip_bf16.h>
#include <stdint.h>

namespace naive {
constexpr int S = 2048, D = 448, F = 1792, FH = 896;
typedef unsigned short u16;
__device__ __forceinline__ float bf2f(u16 b) { return __builtin_bit_cast(float, (unsigned)b << 16); }
__device__ __forceinline__ u16 f2bf(float f) { return __builtin_bit_cast(u16, (__bf16)f); }
__device__ __forceinline__ float rb(float f) { return bf2f(f2bf(f)); }

__global__ void ln_rows(const u16* in, int ld, int n, u16* out, int ldo) {
  int row = blockIdx.x, t = threadIdx.x;
  __shared__ float red[256];
  float s = 0.f;
  for (int i = t; i < n; i += 256) s += bf2f(in[(size_t)row * ld + i]);
  red[t] = s; __syncthreads();
  for (int o = 128; o > 0; o >>= 1) { if (t < o) red[t] += red[t + o]; __syncthreads(); }
  float mean = red[0] / n; __syncthreads();
  float q = 0.f;
  for (int i = t; i < n; i += 256) { float d = bf2f(in[(size_t)row * ld + i]) - mean; q += d * d; }
  red[t] = q; __syncthreads();
  for (int o = 128; o > 0; o >>= 1) { if (t < o) red[t] += red[t + o]; __syncthreads(); }
  float var = red[0] / n;
  float m = rb(mean), v = rb(var);
  float den = rb(sqrtf(rb(v + 1e-5f)));
  for (int i = t; i < n; i += 256) out[(size_t)row * ldo + i] = f2bf(rb(bf2f(in[(size_t)row * ld + i]) - m) / den);
}
__global__ void kv_k(const u16* k, const u16* v, const u16* sk, u16* kv) {
  int i = blockIdx.x * 256 + threadIdx.x; if (i >= S * D) return;
  int s = i / D, d = i % D;
  float acc = 0.f;
  for (int t = 0; t < 4; ++t) {
    int ss = s + t - 3;
    float kk = ss >= 0 ? bf2f(k[(size_t)ss * D + d]) : 0.f;
    acc = rb(acc + rb(kk * bf2f(sk[d * 4 + 3 - t])));
  }
  kv[i] = f2bf(acc * bf2f(v[i]));
}
__global__ void conv_k(const u16* kv, const u16* h, const u16* Dv, u16* y) {
  int i = blockIdx.x * 256 + threadIdx.x; if (i >= S * D) return;
  int s = i / D, d = i % D;
  float acc = 0.f;
  for (int j = 0; j <= s; ++j) acc += bf2f(kv[(size_t)(s - j) * D + d]) * bf2f(h[(size_t)d * S + j]);
  y[i] = f2bf(rb(acc) + rb(bf2f(kv[i]) * bf2f(Dv[d])));
}
__device__ __forceinline__ float gelu_t(float x) {
  float u = 0.7978845608028654f * (x + 0.044715f * x * x * x);
  return 0.5f * x * (1.f + tanhf(u));
}
__global__ void gemm1_k(const u16* xn, const u16* w, u16* z) {
  int i = blockIdx.x * 256 + threadIdx.x; if (i >= S * F) return;
  int s = i / F, f = i % F;
  float acc = 0.f;
  for (int k = 0; k < D; ++k) acc += bf2f(xn[(size_t)s * D + k]) * bf2f(w[(size_t)k * F + f]);
  z[i] = f2bf(gelu_t(rb(acc)));
}
__global__ void gemm2_k(const u16* wsp, const u16* z2n, const u16* b, const u16* z, u16* hh) {
  int i = blockIdx.x * 256 + threadIdx.x; if (i >= S * FH) return;
  int t = i / FH, f = i % FH;
  float acc = 0.f;
  for (int s = 0; s < S; ++s) acc += bf2f(wsp[(size_t)t * S + s]) * bf2f(z2n[(size_t)s * FH + f]);
  float g = rb(rb(acc) + bf2f(b[t]));
  hh[i] = f2bf(bf2f(z[(size_t)t * F + f]) * g);
}
__global__ void gemm3_k(const u16* hh, const u16* wo, const u16* x, const u16* xn, const u16* y, u16* out) {
  int i = blockIdx.x * 256 + threadIdx.x; if (i >= S * D) return;
  int s = i / D, d = i % D;
  float acc = 0.f;
  for (int f = 0; f < FH; ++f) acc += bf2f(hh[(size_t)s * FH + f]) * bf2f(wo[(size_t)f * D + d]);
  float o = rb(acc);
  float g = rb(o / (1.f + expf(-o)));
  float sv = rb(bf2f(xn[i]) * bf2f(y[i]));
  out[i] = f2bf(bf2f(x[i]) + rb(sv * g));
}
static void launch(void* const* d_in, void* d_out, void* d_ws, hipStream_t stream) {
  const u16* q = (const u16*)d_in[0]; const u16* k = (const u16*)d_in[1]; const u16* v = (const u16*)d_in[2];
  const u16* sk = (const u16*)d_in[3]; const u16* h = (const u16*)d_in[4]; const u16* Dv = (const u16*)d_in[5];
  const u16* win = (const u16*)d_in[7]; const u16* wsp = (const u16*)d_in[8]; const u16* bsp = (const u16*)d_in[9];
  const u16* wo = (const u16*)d_in[10];
  u16* ws = (u16*)d_ws;
  u16* xn = ws; u16* kv = xn + S * D; u16* y = kv + S * D; u16* z = y + S * D; u16* z2n = z + S * F; u16* hh = z2n + S * FH;
  ln_rows<<<S, 256, 0, stream>>>(q, D, D, xn, D);
  kv_k<<<(S * D + 255) / 256, 256, 0, stream>>>(k, v, sk, kv);
  conv_k<<<(S * D + 255) / 256, 256, 0, stream>>>(kv, h, Dv, y);
  gemm1_k<<<(S * F + 255) / 256, 256, 0, stream>>>(xn, win, z);
  ln_rows<<<S, 256, 0, stream>>>(z + FH, F, FH, z2n, FH);
  gemm2_k<<<(S * FH + 255) / 256, 256, 0, stream>>>(wsp, z2n, bsp, z, hh);
  gemm3_k<<<(S * D + 255) / 256, 256, 0, stream>>>(hh, wo, q, xn, y, (u16*)d_out);
}
}

extern "C" void kernel_launch(void* const* d_in, const int* in_sizes, int n_in,
                              void* d_out, int out_size, void* d_ws, size_t ws_size,
                              hipStream_t stream) {
  naive::launch(d_in, d_out, d_ws, stream);
}
```

```cpp
#include <hip/hip_runtime.h>
#include <stdint.h>

namespace fk {
typedef unsigned u32;
typedef unsigned long long u64;
typedef __attribute__((ext_vector_type(8))) __bf16 bf8;
typedef __attribute__((ext_vector_type(2))) __bf16 bf2;
typedef __attribute__((ext_vector_type(4))) u32 u4;
typedef __attribute__((ext_vector_type(2))) u32 u2;
typedef __attribute__((ext_vector_type(2))) float f2;
typedef __attribute__((ext_vector_type(4))) float f4;
typedef __attribute__((ext_vector_type(16))) float f16v;
typedef __attribute__((ext_vector_type(4))) short s4;
typedef u4 __attribute__((aligned(8))) u4a8;
typedef __amdgpu_buffer_rsrc_t rsrc_t;
#define LDSP __attribute__((address_space(3)))
#define DEV __device__ __forceinline__
#define FENCE() asm volatile("" ::: "memory")
#define SB() __builtin_amdgcn_sched_barrier(0)
#define PINA(x) asm("" : "+a"(x))
constexpr unsigned OOB = 0x80000000u;

constexpr u32 WS_E = 0;
constexpr u32 WS_FZ = 4096;
constexpr u32 WS_FH = 16384;
constexpr u32 WS_FK = 32768;
constexpr u32 WS_FY = 36864;
constexpr u32 WS_ST = 65536;
constexpr u32 WS_Z = 1u << 20;
constexpr u32 WS_H = 5u << 20;
constexpr u32 WS_KV = 9u << 20;
constexpr u32 WS_Y = 11u << 20;
constexpr u32 WS_END = 13u << 20;
#define STAMP_OFF (16u << 20)
constexpr int SC1 = 16;
constexpr u32 SPIN = 1u << 18;
constexpr u32 CODE_BYTES = 46080;

__shared__ __attribute__((aligned(16))) char smem[163840];

template <int V> struct IC { static constexpr int v = V; };
template <int I, int N, class F> DEV void sfor(F&& f) { if constexpr (I < N) { f(IC<I>{}); sfor<I + 1, N>(f); } }
DEV u32 opq(u32 x) { asm("" : "+v"(x)); return x; }
DEV u32 opqs(u32 x) { asm("" : "+s"(x)); return x; }
DEV u4 pina(u4 x) { PINA(x); return x; }
DEV LDSP char* lp(u32 off) { return (LDSP char*)smem + off; }
DEV u4 lr128(u32 off) { return *(LDSP u4*)lp(off); }
DEV u4 lr128a8(u32 off) { return *(LDSP u4a8*)lp(off); }
DEV u2 lr64(u32 off) { return *(LDSP u2*)lp(off); }
DEV void lw128(u32 off, u4 v) { *(LDSP u4*)lp(off) = v; }
DEV void lw64(u32 off, u2 v) { *(LDSP u2*)lp(off) = v; }
DEV void lw32(u32 off, u32 v) { *(LDSP u32*)lp(off) = v; }
DEV u2 ltr(u32 off) { return __builtin_bit_cast(u2, __builtin_amdgcn_ds_read_tr16_b64_v4i16((LDSP s4*)lp(off))); }
DEV u4 ltr2(u32 a, u32 b) { u2 x = ltr(a), y = ltr(b); return (u4){x[0], x[1], y[0], y[1]}; }
DEV void bar() { asm volatile("s_waitcnt lgkmcnt(0)\n\ts_barrier" ::: "memory"); }
DEV void drain() { asm volatile("s_waitcnt vmcnt(0)" ::: "memory"); }
DEV rsrc_t mk(const void* p, u32 n) { return __builtin_amdgcn_make_buffer_rsrc((void*)p, 0, n, 0x00020000); }
template <int AUX> DEV u4 ld128(rsrc_t r, u32 v, u32 s) { return __builtin_amdgcn_raw_buffer_load_b128(r, v, s, AUX); }
template <int AUX> DEV u2 ld64(rsrc_t r, u32 v, u32 s) { return __builtin_amdgcn_raw_buffer_load_b64(r, v, s, AUX); }
template <int AUX> DEV void st128(u4 d, rsrc_t r, u32 v) { __builtin_amdgcn_raw_buffer_store_b128(d, r, v, 0, AUX); }
template <int AUX> DEV void st64(u2 d, rsrc_t r, u32 v, u32 s) { __builtin_amdgcn_raw_buffer_store_b64(d, r, v, s, AUX); }
DEV f16v mm32(u4 a, u4 b, f16v c) { return __builtin_amdgcn_mfma_f32_32x32x16_bf16(__builtin_bit_cast(bf8, a), __builtin_bit_cast(bf8, b), c, 0, 0, 0); }
DEV f4 mm16(u4 a, u4 b, f4 c) { return __builtin_amdgcn_mfma_f32_16x16x32_bf16(__builtin_bit_cast(bf8, a), __builtin_bit_cast(bf8, b), c, 0, 0, 0); }
DEV u32 pk(float lo, float hi) { return __builtin_bit_cast(u32, __builtin_convertvector((f2){lo, hi}, bf2)); }
DEV float asf(u32 d) { return __builtin_bit_cast(float, d); }
DEV u32 asu(float d) { return __builtin_bit_cast(u32, d); }
DEV float blo(u32 d) { return __builtin_bit_cast(float, d << 16); }
DEV float bhi(u32 d) { return __builtin_bit_cast(float, d & 0xffff0000u); }
DEV f2 up(u32 d) { return (f2){blo(d), bhi(d)}; }
DEV float dot2(u32 a, u32 b, float c) { return __builtin_amdgcn_fdot2_f32_bf16(__builtin_bit_cast(bf2, a), __builtin_bit_cast(bf2, b), c, false); }
DEV float xhalf(float v) {
  u32 x = __builtin_bit_cast(u32, v);
  auto r = __builtin_amdgcn_permlane32_swap(x, x, false, false);
  return __builtin_bit_cast(float, (u32)r[0]) + __builtin_bit_cast(float, (u32)r[1]);
}
DEV bool allok(bool c) { return __builtin_amdgcn_ballot_w64(!c) == 0; }
DEV void wait_flags(rsrc_t r, u32 voff, u2 tag) {
#pragma nounroll
  for (u32 it = 0; it < SPIN; ++it) {
    FENCE();
    u2 f = ld64<SC1>(r, voff, 0);
    if (allok(f[0] == tag[0] && f[1] == tag[1])) break;
    __builtin_amdgcn_s_sleep(1);
  }
  FENCE();
}

#define STAMP(k)

DEV void gelu32(f16v (&a)[2], float ca, float cb, float inv) {
#pragma unroll
  for (int n = 0; n < 2; ++n)
#pragma unroll
    for (int r = 0; r < 16; r += 2) {
      f2 p = {a[n][r], a[n][r + 1]};
      f2 t = p * (p * p * cb + ca);
      f2 e = {__builtin_amdgcn_exp2f(t[0]), __builtin_amdgcn_exp2f(t[1])};
      f2 d = e * inv + inv;
      f2 q = {__builtin_amdgcn_rcpf(d[0]), __builtin_amdgcn_rcpf(d[1])};
      p = p * q;
      a[n][r] = p[0]; a[n][r + 1] = p[1];
    }
}

DEV void conv_pair(f4 (&acc)[8], u32 ha, u32 ua) {
#pragma unroll
  for (int rr = 0; rr < 2; ++rr) {
    u4 A[8], B[8];
#pragma unroll
    for (int a = 0; a < 8; ++a) { A[a] = lr128a8(ha + 512 * a + 64 * rr); B[a] = lr128(ua + 512 * (7 - a) + 64 * rr); }
#pragma unroll
    for (int m = 0; m < 8; ++m)
#pragma unroll
      for (int a = 0; a <= m; ++a) {
        if (a + m < 8) acc[a + m] = mm16(A[a], B[m], acc[a + m]);
        if (a != m && a + m < 8) acc[a + m] = mm16(A[m], B[a], acc[a + m]);
      }
  }
}
DEV void put_copies(u32 base, u4 d, u2 p) {
  u32 e0 = __builtin_amdgcn_alignbit(p[1], p[0], 16), e1 = __builtin_amdgcn_alignbit(d[0], p[1], 16);
  u32 e2 = __builtin_amdgcn_alignbit(d[1], d[0], 16), e3 = __builtin_amdgcn_alignbit(d[2], d[1], 16);
  u32 e4 = __builtin_amdgcn_alignbit(d[3], d[2], 16);
  lw128(base, (u4){e0, e1, e2, e3});
  lw128(base + 4128, (u4){p[1], d[0], d[1], d[2]});
  lw128(base + 8256, (u4){e1, e2, e3, e4});
  lw128(base + 12384, d);
}

__global__ __launch_bounds__(256, 1) void fused(const void* __restrict__ gx, const void* __restrict__ gk, const void* __restrict__ gv,
                                                const void* __restrict__ gsk, const void* __restrict__ gh, const void* __restrict__ gD,
                                                const void* __restrict__ gwin, const void* __restrict__ gwsp, const void* __restrict__ gb,
                                                const void* __restrict__ gwo, void* __restrict__ gout, void* __restrict__ gws) {
  STAMP(0);
  const u32 tid = threadIdx.x, l = tid & 63;
  const rsrc_t rC = mk((const void*)(__builtin_amdgcn_s_getpc() & ~255ull), CODE_BYTES);
  u32 pf0 = __builtin_amdgcn_raw_buffer_load_b32(rC, tid * 128, 0, 0), pf1 = __builtin_amdgcn_raw_buffer_load_b32(rC, tid * 128, 32768, 0);
  SB();
  const u32 w = __builtin_amdgcn_readfirstlane(tid >> 6);
  const u32 b = blockIdx.x, xcd = b & 7, j = b >> 3;
  const rsrc_t rW = mk(gws, WS_END);
  const rsrc_t rH = mk(gh, 448 * 4096);
  const u2 e0 = ld64<SC1>(rW, 0, WS_E);
  const u32 i32 = l & 31, hh = l >> 5, i16 = l & 15, g4 = l >> 4;
  u2 tag;
  const u32 l16 = opq(l * 16);

  if (j < 28) {
    const u32 pl = j / 7, cl = j - pl * 7, p = 4 * (xcd >> 1) + pl, half = xcd & 1, c = 7 * half + cl;
    const rsrc_t rX = mk(gx, 2048 * 896), rWi = mk(gwin, 448 * 3584), rS = mk(gwsp, 2048 * 4096);
    u4 xr[28], w2r[14], w1r[14];
    {
      const u32 vx = (l >> 3) * 896 + (l & 7) * 16, sx = (128 * p + 32 * w) * 896;
#pragma unroll
      for (int kc = 0; kc < 7; ++kc)
#pragma unroll
        for (int rb = 0; rb < 4; ++rb) { xr[kc * 4 + rb] = ld128<0>(rX, vx + kc * 128, sx + rb * 7168); SB(); }
      const u32 vw = (l >> 3) * 3584 + (l & 7) * 16, sw = w * 8 * 3584 + c * 128;
#pragma unroll
      for (int n = 0; n < 14; ++n) { w2r[n] = ld128<0>(rWi, vw, sw + 1792 + n * 32 * 3584); SB(); }
#pragma unroll
      for (int n = 0; n < 14; ++n) { w1r[n] = ld128<0>(rWi, vw, sw + n * 32 * 3584); SB(); }
    }
    STAMP(16);
    const u32 wrA0 = (l >> 3) * 128 + (((l & 7) ^ (l >> 4)) << 4), wrA1 = wrA0 ^ 64;
    const u32 rdA = i32 * 128 + ((hh ^ ((i32 >> 1) & 7)) << 4);
    u4 xf[28];
    float sx1 = 0.f, sx2 = 0.f;
    {
      const u32 stg = 131072 + w * 8192;
      const u32 wa[2] = {opq(stg + wrA0), opq(stg + wrA1)};
      u32 ra[4];
#pragma unroll
      for (int q = 0; q < 4; ++q) ra[q] = opq(stg + (rdA ^ ((q ^ w) << 5)));
#pragma unroll
      for (int kc = 0; kc < 8; ++kc) {
        if (kc < 7) {
#pragma unroll
          for (int rb = 0; rb < 4; ++rb) lw128(wa[rb & 1] + (kc & 1) * 4096 + rb * 1024, pina(xr[kc * 4 + rb]));
          FENCE();
#pragma unroll
          for (int q = 0; q < 4; ++q) xf[kc * 4 + q] = lr128(ra[q] + (kc & 1) * 4096);
          FENCE();
        }
        if (kc == 0) STAMP(17);
        if (kc == 3) STAMP(18);
        if (kc == 6) STAMP(19);
        if (kc > 0)
#pragma unroll
          for (int n = 4 * kc - 4; n < 4 * kc; ++n)
#pragma unroll
            for (int d = 0; d < 4; ++d) { sx1 = dot2(xf[n][d], 0x3f803f80u, sx1); sx2 = dot2(xf[n][d], xf[n][d], sx2); }
        SB();
      }
    }
    {
      u64 e = ((u64)__builtin_amdgcn_readfirstlane(e0[1]) << 32) | __builtin_amdgcn_readfirstlane(e0[0]);
      e = e * 6364136223846793005ull + 1442695040888963407ull;
      tag = (u2){(u32)e, (u32)(e >> 32)};
      asm volatile("" ::"v"(pf0), "v"(pf1));
    }
    const u32 wrW = opq(w * 1024 + (l >> 3) * 128 + (((l & 7) ^ (((l >> 4) & 1) << 2)) << 4)), wrW1 = opq(wrW + 65536);
    const u32 pw = opqs(p * 4 + w);
    float bt = blo(((const unsigned short*)gb)[128 * p + 32 * w + i32]);
#pragma unroll
    for (int n = 0; n < 14; ++n) lw128(n * 4096 + wrW, pina(w2r[n]));
    sx1 = xhalf(sx1); sx2 = xhalf(sx2);
    const float mean = sx1 * (1.f / 448.f);
    const float var = fmaxf(sx2 * (1.f / 448.f) - mean * mean, 0.f) + 1e-5f;
    const float rstd = __builtin_amdgcn_rsqf(var), sdev = var * rstd;
    u4 mop;
    {
      u32 mh = pk(-mean, -mean);
      u32 ml = pk(-mean - blo(mh), 0.f);
      mop = hh ? (u4){0, 0, 0, 0} : (u4){mh, ml, 0, 0};
    }
    const float ca = -2.302208198f * rstd, cb = -0.1029432397f * rstd * rstd * rstd;
    const u32 selv = ((g4 ^ i16) & 1) ? 0u : 0x3f803f80u;
    const u4 sel = {selv, selv, selv, selv};
    const u32 trA = (8 * hh + ((l >> 2) & 3)) * 128 + (((g4 & 1) * 32 + (l & 3) * 8) ^ (((l >> 3) & 1) << 6));
    STAMP(1);
    bar();
    STAMP(2);
    const u32 vS = (l >> 3) * 4096 + (l & 7) * 16;
    const u32 kb0 = 8 * ((w + pl) & 3);
    auto blk = [&](int n) -> u32 { return kb0 + ((n + cl) & 7); };
    auto ldS = [&](int n, int m) -> u4 { return ld128<0>(rS, vS, (128 * p + 32 * (((m >> 2) + w + 1) & 3) + 8 * (m & 3)) * 4096 + blk(n) * 128); };
    u4 R[8][16];
    f16v z2[2], z1[2];
    auto gemm1 = [&](f16v (&acc)[2], u32 img, auto&& side) {
      f4 cs[2] = {(f4)0.f, (f4)0.f};
      acc[0] = (f16v)0.f; acc[1] = (f16v)0.f;
      u4 wf[2][2];
      u32 ta[4][2];
#pragma unroll
      for (int q = 0; q < 4; ++q) { ta[q][0] = opq(img + trA + ((q ^ w) << 11)); ta[q][1] = opq(ta[q][0] ^ 64); }
      auto rd = [&](int s, u4 (&o)[2]) {
        const u32 a = ta[s & 3][0] + (s >> 2) * 8192, a1 = ta[s & 3][1] + (s >> 2) * 8192;
        o[0] = ltr2(a, a + 512); o[1] = ltr2(a1, a1 + 512);
      };
      rd(0, wf[0]);
#pragma unroll
      for (int s = 0; s < 28; ++s) {
        if (s + 1 < 28) rd(s + 1, wf[(s + 1) & 1]);
        acc[0] = mm32(wf[s & 1][0], xf[s], acc[0]);
        SB();
        side(s);
        acc[1] = mm32(wf[s & 1][1], xf[s], acc[1]);
        if ((s & 3) == 0) { cs[0] = mm16(sel, wf[s & 1][0], cs[0]); cs[1] = mm16(sel, wf[s & 1][1], cs[1]); }
        SB();
      }
      if (img) STAMP(22); else STAMP(20);
      float cv = (g4 & 2) ? ((g4 & 1) ? cs[1][1] : cs[1][0]) : ((g4 & 1) ? cs[0][1] : cs[0][0]);
      const u32 ex = 135168 + (img ? 1024 : 0);
      lw32(ex + l * 16 + w * 4, __builtin_bit_cast(u32, cv));
      bar();
#pragma unroll
      for (int n = 0; n < 2; ++n) {
        f4 t = __builtin_bit_cast(f4, lr128(ex + (32 * n + i32) * 16));
        float s = (t[0] + t[1]) + (t[2] + t[3]);
        u32 ch = pk(s, 0.f);
        u32 cc = pk(s, s - blo(ch));
        acc[n] = mm32((u4){cc, ch, 0, 0}, mop, acc[n]);
      }
      if (img) STAMP(23); else STAMP(21);
      gelu32(acc, ca, cb, sdev);
    };
    gemm1(z2, 0, [&](int s) {
      if (s >= 14) lw128((s - 14) * 4096 + wrW1, pina(w1r[s - 14]));
      if (s < 16) R[0][s] = ldS(0, s);
      if (s >= 12) R[1][s - 12] = ldS(1, s - 12);
    });
    STAMP(3);
    {
      f2 a1 = {0.f, 0.f}, a2 = {0.f, 0.f};
#pragma unroll
      for (int n = 0; n < 2; ++n)
#pragma unroll
        for (int r = 0; r < 16; r += 2) { f2 v = {z2[n][r], z2[n][r + 1]}; a1 += v; a2 += v * v; }
      float s1 = xhalf(a1[0] + a1[1]), s2 = xhalf(a2[0] + a2[1]);
      st128<SC1>((u4){__builtin_bit_cast(u32, s1), tag[0], __builtin_bit_cast(u32, s2), tag[1]}, rW, (hh ? OOB : i32 * 16) + (WS_ST + (pw * 14 + c) * 512));
    }
    SB();
    const u32 ws0[2] = {opq(w * 16384 + wrA0), opq(w * 16384 + wrA1)}, ws1[2] = {opq(ws0[0] + 65536), opq(ws0[1] + 65536)};
    gemm1(z1, 65536, [&](int s) {
      if (s < 16) R[2][s] = ldS(2, s);
      if (s >= 12) lw128(ws0[s & 1] + (s - 12) * 1024, pina(R[0][s - 12]));
    });
    u32 z1p[16];
#pragma unroll
    for (int r = 0; r < 16; ++r) { z1p[r] = pk(z1[r >> 3][(2 * r) & 15], z1[r >> 3][(2 * r + 1) & 15]); asm volatile("" : "+v"(z1p[r])); }
    asm volatile("" : "+v"(bt));
    STAMP(4);
    {
      float s1, s2;
#pragma nounroll
      for (u32 it = 0;; ++it) {
        FENCE();
        u4 gq[7];
#pragma unroll
        for (int n = 0; n < 7; ++n) gq[n] = ld128<SC1>(rW, hh * 3584 + i32 * 16 + n * 512, WS_ST + pw * 14 * 512);
        u32 bad = 0; s1 = 0.f; s2 = 0.f;
#pragma unroll
        for (int n = 0; n < 7; ++n) { bad |= (gq[n][1] ^ tag[0]) | (gq[n][3] ^ tag[1]); s1 += asf(gq[n][0]); s2 += asf(gq[n][2]); }
        if (allok(bad == 0) || it >= SPIN) break;
        __builtin_amdgcn_s_sleep(1);
      }
      STAMP(5);
      s1 = xhalf(s1) * (1.f / 896.f); s2 = xhalf(s2) * (1.f / 896.f);
      const float r2 = __builtin_amdgcn_rsqf(fmaxf(s2 - s1 * s1, 0.f) + 1e-5f), o2 = -s1 * r2;
      const u32 tb = 131072 + w * 8192;
      const u32 sg = (i32 & 5) | ((i32 & 2) << 2) | ((i32 & 8) >> 2);
      const u32 twr = tb + i32 * 128 + ((hh ^ sg) << 3);
#pragma unroll
      for (int n = 0; n < 2; ++n)
#pragma unroll
        for (int rq = 0; rq < 4; ++rq) {
          f2 a = {z2[n][4 * rq], z2[n][4 * rq + 1]}, bq = {z2[n][4 * rq + 2], z2[n][4 * rq + 3]};
          a = a * r2 + o2; bq = bq * r2 + o2;
          lw64(twr ^ ((8 * n + 2 * rq) << 3), (u2){pk(a[0], a[1]), pk(bq[0], bq[1])});
        }
      FENCE();
      const u32 qq = (l >> 2) & 3;
      const u32 trd = tb + (8 * hh + qq) * 128 + (((4 * (g4 & 1) + (l & 3)) ^ ((qq & 1) | ((qq & 2) << 2) | (hh << 1))) << 3);
      const u32 zo = opq(l * 16 + WS_Z + (c * 64 + pw) * 4096);
#pragma unroll
      for (int q = 0; q < 2; ++q)
#pragma unroll
        for (int n = 0; n < 2; ++n) {
          const u32 a = (trd ^ (n << 6)) + q * 2048;
          st128<SC1>(ltr2(a, (a ^ 32) + 512), rW, zo + (q * 2 + n) * 1024);
        }
      drain();
      st64<SC1>(tag, rW, l ? OOB : 0, WS_FZ + (c * 64 + pw) * 8);
    }
    STAMP(6);
#pragma unroll
    for (int m = 0; m < 16; ++m) { R[3][m] = ldS(3, m); SB(); }
#pragma unroll
    for (int m = 0; m < 16; ++m) lw128(ws1[m & 1] + m * 1024, pina(R[1][m]));
    const u32 mi = xcd * 28 + j;
    if (mi < 192) {
      const u32 ch = 256 + mi;
      u4 hd = ld128<0>(rH, tid * 16, ch * 4096);
      u2 hp = ld64<0>(rH, tid ? tid * 16 - 8 : OOB, ch * 4096);
      const float dv = blo(((const unsigned short*)gD)[ch]);
      wait_flags(rW, WS_FK + i32 * 8, tag);
      STAMP(24);
      u4 ud = ld128<SC1>(rW, (255 - tid) * 16, WS_KV + ch * 4096);
      bar();
      const u32 cb0 = 131072;
      put_copies(cb0 + 32 + tid * 16, hd, hp);
      if (tid < 8) lw128(cb0 + (tid >> 1) * 4128 + (tid & 1) * 16, (u4){0, 0, 0, 0});
      if (tid < 32) lw128(cb0 + 16512 + 4096 + tid * 16, (u4){0, 0, 0, 0});
      lw128(cb0 + 16512 + (255 - tid) * 16, ud);
      bar();
      STAMP(25);
      f4 acc[8];
#pragma unroll
      for (int n = 0; n < 8; ++n) acc[n] = (f4)0.f;
      conv_pair(acc, opq(cb0 + (i16 & 3) * 4128 + 8 + 16 * g4 + 8 * (i16 >> 2) + 128 * w), opq(cb0 + 16512 + 480 - 32 * i16 + 16 * g4 + 128 * w));
      bar();
#pragma unroll
      for (int n = 0; n < 8; ++n) lw128(cb0 + w * 8192 + n * 1024 + i16 * 64 + ((g4 ^ ((i16 >> 1) & 3)) << 4), __builtin_bit_cast(u4, acc[n]));
      bar();
      STAMP(26);
      f4 y0 = (f4)0.f, y1 = (f4)0.f;
      const u32 ra = cb0 + ((2 * tid) ^ ((tid >> 2) & 3)) * 16;
#pragma unroll
      for (int n = 0; n < 4; ++n) { y0 += __builtin_bit_cast(f4, lr128(ra + n * 8192)); y1 += __builtin_bit_cast(f4, lr128((ra ^ 16) + n * 8192)); }
      u4 o;
      o[0] = pk(y0[0] + bhi(ud[3]) * dv, y0[1] + blo(ud[3]) * dv); o[1] = pk(y0[2] + bhi(ud[2]) * dv, y0[3] + blo(ud[2]) * dv);
      o[2] = pk(y1[0] + bhi(ud[1]) * dv, y1[1] + blo(ud[1]) * dv); o[3] = pk(y1[2] + bhi(ud[0]) * dv, y1[3] + blo(ud[0]) * dv);
      st128<SC1>(o, rW, tid * 16 + WS_Y + ch * 4096);
      drain();
      bar();
      st64<SC1>(tag, rW, tid ? OOB : 0, WS_FY + ch * 8);
    }
#pragma unroll
    for (int m = 0; m < 16; ++m) { R[4][m] = ldS(4, m); SB(); }
    STAMP(7);
    wait_flags(rW, WS_FZ + (c * 64 + 2 * kb0 + i16) * 8, tag);
    STAMP(8);
    u4 Z[8][8];
    auto ldZ = [&](int n, int m) -> u4 { return ld128<SC1>(rW, l16 + (m & 3) * 1024, WS_Z + (c * 128 + 4 * blk(n)) * 2048 + (m >> 2) * 4096); };
#pragma unroll
    for (int n = 0; n < 3; ++n)
#pragma unroll
      for (int m = 0; m < 8; ++m) { Z[n][m] = ldZ(n, m); SB(); }
    f16v acc[4][2];
#pragma unroll
    for (int t = 0; t < 4; ++t) { acc[t][0] = (f16v)0.f; acc[t][1] = (f16v)0.f; }
    u4 WO[28];
    const rsrc_t rO = mk(gwo, 896 * 896);
    const u32 vO = (l >> 3) * 896 + (l & 7) * 16;
    {
      u4 Bf[2][4];
      const u32 sb = w * 16384;
      u32 rq[2][4];
#pragma unroll
      for (int q = 0; q < 4; ++q) { rq[0][q] = opq(sb + (rdA ^ (q << 5))); rq[1][q] = opq(rq[0][q] + 65536); }
#pragma unroll
      for (int q = 0; q < 4; ++q) Bf[0][q] = lr128(rq[0][q]);
      SB();
      auto issue = [&](auto kk) {
        constexpr int k = decltype(kk)::v;
        if constexpr (k < 0) return;
        else if constexpr (k < 8) Z[3][k] = ldZ(3, k);
        else if constexpr (k < 24) R[5][k - 8] = ldS(5, k - 8);
        else if constexpr (k < 32) Z[4][k - 24] = ldZ(4, k - 24);
        else if constexpr (k < 48) R[6][k - 32] = ldS(6, k - 32);
        else if constexpr (k < 56) Z[5][k - 48] = ldZ(5, k - 48);
        else if constexpr (k < 72) R[7][k - 56] = ldS(7, k - 56);
        else if constexpr (k < 80) Z[6][k - 72] = ldZ(6, k - 72);
        else if constexpr (k < 88) Z[7][k - 80] = ldZ(7, k - 80);
        else if constexpr (k < 116) WO[k - 88] = ld128<0>(rO, vO, (224 * w + 8 * (k - 88)) * 896 + cl * 128);
      };
      sfor<0, 128>([&](auto ii) {
        constexpr int gi = decltype(ii)::v >> 2, q = decltype(ii)::v & 3;
        constexpr int bb = gi >> 2, t = gi & 3, gn = gi + 1;
        constexpr int wb = (gi >= 1 && gi <= 12) ? ((gi - 1) >> 2) + 2 : ((gi >= 16 && gi < 28) ? bb + 1 : -1);
        constexpr int wt = (gi <= 12) ? (gi - 1) & 3 : t;
        if constexpr (gn < 32) Bf[gn & 1][q] = lr128(rq[(gn >> 2) & 1][q] + (gn & 3) * 4096);
        if constexpr (gi == 12) { PINA(Z[3][2 * q]); PINA(Z[3][2 * q + 1]); }
        issue(IC<4 * (gi - 2) + q>{});
        acc[t][0] = mm32(Z[bb][2 * q], Bf[gi & 1][q], acc[t][0]);
        SB();
        if constexpr (wb >= 0) {
          if constexpr (wb != 4 && wb != 6) PINA(R[wb][4 * wt + q]);
          lw128(((wb & 1) ? ws1[q & 1] : ws0[q & 1]) + (4 * wt + q) * 1024, R[wb][4 * wt + q]);
        }
        acc[t][1] = mm32(Z[bb][2 * q + 1], Bf[gi & 1][q], acc[t][1]);
        SB();
        if constexpr (q == 0 && t == 0) STAMP(32 + bb);
      });
    }
    STAMP(9);
    {
      bar();
#pragma unroll
      for (int t = 0; t < 3; ++t)
#pragma unroll
        for (int n = 0; n < 8; ++n) {
          f4 v = {acc[t][n >> 2][4 * (n & 3)], acc[t][n >> 2][4 * (n & 3) + 1], acc[t][n >> 2][4 * (n & 3) + 2], acc[t][n >> 2][4 * (n & 3) + 3]};
          lw128(w * 24576 + t * 8192 + n * 1024 + l * 16, __builtin_bit_cast(u4, v));
        }
      lw64(131072 + (32 * w + i32) * 8, (u2){__builtin_bit_cast(u32, mean), __builtin_bit_cast(u32, rstd)});
      bar();
#pragma unroll
      for (int t = 0; t < 3; ++t) {
        const u32 a = ((w - t - 1) & 3) * 24576 + t * 8192 + l * 16;
#pragma unroll
        for (int n = 0; n < 8; ++n) {
          f4 v = __builtin_bit_cast(f4, lr128(a + n * 1024));
#pragma unroll
          for (int r = 0; r < 4; ++r) acc[3][n >> 2][4 * (n & 3) + r] += v[r];
        }
      }
      STAMP(27);
      const u32 ho = opq(l * 16 + WS_H + (pw * 56 + 4 * c) * 1024);
#pragma unroll
      for (int n = 0; n < 4; ++n) {
        u4 o;
#pragma unroll
        for (int d = 0; d < 4; ++d) {
          f2 gg = {acc[3][n >> 1][8 * (n & 1) + 2 * d], acc[3][n >> 1][8 * (n & 1) + 2 * d + 1]};
          gg = (gg + bt) * up(z1p[4 * n + d]);
          o[d] = pk(gg[0], gg[1]);
        }
        st128<SC1>(o, rW, ho + n * 1024);
      }
      drain();
      st64<SC1>(tag, rW, l ? OOB : 0, WS_FH + (pw * 16 + c) * 8);
    }
    STAMP(10);
    bar();
    {
      const u32 ob = w * 28672;
      const u32 wrO = opq(ob + (l >> 3) * 128 + (((l & 7) ^ (((l >> 4) & 1) << 2)) << 4));
#pragma unroll
      for (int n = 0; n < 28; ++n) lw128(wrO + n * 1024, pina(WO[n]));
      FENCE();
      const u32 tro = opq(ob + (4 * hh + ((l >> 2) & 3)) * 128 + (((g4 & 1) * 32 + (l & 3) * 8) ^ ((((l >> 3) ^ w) & 1) << 6))), tro1 = opq(tro ^ 64);
      u4 Af[14][2];
#pragma unroll
      for (int n = 0; n < 14; ++n) { Af[n][0] = ltr2(tro + n * 2048, tro + n * 2048 + 1024); Af[n][1] = ltr2(tro1 + n * 2048, tro1 + n * 2048 + 1024); }
      STAMP(28);
      const u32 T0 = 128 * p + 64 * half + 32 * (w >> 1), D0 = 64 * cl + 32 * (w & 1);
      u2 xv[4];
#pragma unroll
      for (int r = 0; r < 4; ++r) xv[r] = ld64<0>(rX, i32 * 896 + hh * 8 + r * 16, T0 * 896 + D0 * 2);
      wait_flags(rW, WS_FY + (D0 + i32) * 8, tag);
      STAMP(29);
      u4 yr[2];
#pragma unroll
      for (int n = 0; n < 2; ++n) yr[n] = ld128<SC1>(rW, (l >> 2) * 4096 + (l & 3) * 16, WS_Y + (D0 + 16 * n) * 4096 + T0 * 2);
      const u32 yb = 132096 + w * 2048;
#pragma unroll
      for (int n = 0; n < 2; ++n) lw128(yb + n * 1024 + l * 16, yr[n]);
      FENCE();
      const u2 ms = lr64(131072 + (64 * half + 32 * (w >> 1) + i32) * 8);
      const float rs = asf(ms[1]), mo = -asf(ms[0]) * rs;
      f2 sv[8];
#pragma unroll
      for (int r = 0; r < 4; ++r) {
        u2 yy = ltr(yb + (8 * r + 4 * hh + ((l >> 2) & 3)) * 64 + (g4 & 1) * 32 + (l & 3) * 8);
        sv[2 * r] = (up(xv[r][0]) * rs + mo) * up(yy[0]);
        sv[2 * r + 1] = (up(xv[r][1]) * rs + mo) * up(yy[1]);
        asm volatile("" : "+v"(sv[2 * r]), "+v"(sv[2 * r + 1]));
      }
      STAMP(11);
      wait_flags(rW, WS_FH + ((p * 4 + 2 * half + (g4 & 1)) * 16 + (i16 < 14 ? i16 : 0)) * 8, tag);
      STAMP(12);
      u4 Hf[2][14];
#pragma unroll
      for (int t = 1; t >= 0; --t)
#pragma unroll
        for (int n = 0; n < 14; ++n) Hf[t][n] = ld128<SC1>(rW, l16 + (n & 3) * 1024, WS_H + ((p * 4 + 2 * half + (t ^ (w >> 1))) * 56 + 14 * w + (n & 12)) * 1024), SB();
      f16v o[4];
      bar();
#pragma unroll
      for (int lam = 3; lam >= 0; --lam) {
        o[lam] = (f16v)0.f;
#pragma unroll
        for (int n = 0; n < 14; ++n) { if (lam & 1) PINA(Hf[lam >> 1][n]); o[lam] = mm32(Af[n][lam & 1], Hf[lam >> 1][n], o[lam]); }
        if (lam)
#pragma unroll
          for (int n = 0; n < 4; ++n) {
            f4 v = {o[lam][4 * n], o[lam][4 * n + 1], o[lam][4 * n + 2], o[lam][4 * n + 3]};
            lw128(w * 16384 + lam * 4096 + n * 1024 + l * 16, __builtin_bit_cast(u4, v));
          }
      }
      bar();
      STAMP(13);
#pragma unroll
      for (int lam = 1; lam < 4; ++lam)
#pragma unroll
        for (int n = 0; n < 4; ++n) {
          f4 v = __builtin_bit_cast(f4, lr128((w ^ lam) * 16384 + lam * 4096 + n * 1024 + l * 16));
#pragma unroll
          for (int r = 0; r < 4; ++r) o[0][4 * n + r] += v[r];
        }
      const rsrc_t rOut = mk(gout, 2048 * 896);
#pragma unroll
      for (int r = 0; r < 4; ++r) {
        u2 ov;
#pragma unroll
        for (int d = 0; d < 2; ++d) {
          f2 v = {o[0][4 * r + 2 * d], o[0][4 * r + 2 * d + 1]};
          f2 t = v * -1.442695041f;
          f2 e = {__builtin_amdgcn_exp2f(t[0]), __builtin_amdgcn_exp2f(t[1])};
          e = e + 1.f;
          f2 q = {__builtin_amdgcn_rcpf(e[0]), __builtin_amdgcn_rcpf(e[1])};
          v = v * q * sv[2 * r + d] + up(xv[r][d]);
          ov[d] = pk(v[0], v[1]);
        }
        st64<0>(ov, rOut, i32 * 896 + hh * 8 + r * 16, T0 * 896 + D0 * 2);
      }
    }
    if (b == 0) st64<SC1>(tag, rW, tid ? OOB : 0, WS_E);
  } else {
    const u32 sp = xcd * 4 + (j - 28);
    const rsrc_t rK = mk(gk, 2048 * 896), rV = mk(gv, 2048 * 896), rSk = mk(gsk, 448 * 8);
    const u32 R0 = 64 * sp + 16 * w;
    const u32 lv = l < 56 ? l * 16 : 0x80000000u;
    u4 kr[19], vr[16], sk[4];
#pragma unroll
    for (int n = 0; n < 4; ++n) sk[n] = ld128<0>(rSk, l < 56 ? l * 64 + n * 16 : 0x80000000u, 0);
#pragma unroll
    for (int n = 0; n < 3; ++n) kr[n] = ld128<0>(rK, R0 ? lv + n * 896 : 0x80000000u, R0 ? (R0 - 3) * 896 : 0);
#pragma unroll
    for (int n = 0; n < 16; ++n) { kr[n + 3] = ld128<0>(rK, lv + (n & 3) * 896, (R0 + (n & 12)) * 896); vr[n] = ld128<0>(rV, lv + (n & 3) * 896, (R0 + (n & 12)) * 896); SB(); }
    SB();
    {
      u64 e = ((u64)__builtin_amdgcn_readfirstlane(e0[1]) << 32) | __builtin_amdgcn_readfirstlane(e0[0]);
      e = e * 6364136223846793005ull + 1442695040888963407ull;
      tag = (u2){(u32)e, (u32)(e >> 32)};
      asm volatile("" ::"v"(pf0), "v"(pf1));
    }
    STAMP(1);
#pragma unroll
    for (int cp = 0; cp < 4; ++cp) {
      const f2 t0 = {blo(sk[cp][0]), blo(sk[cp][2])}, t1 = {bhi(sk[cp][0]), bhi(sk[cp][2])};
      const f2 t2 = {blo(sk[cp][1]), blo(sk[cp][3])}, t3 = {bhi(sk[cp][1]), bhi(sk[cp][3])};
      f2 kf[19], o[16];
#pragma unroll
      for (int n = 0; n < 19; ++n) kf[n] = up(kr[n][cp]);
#pragma unroll
      for (int n = 0; n < 16; ++n) o[n] = (kf[n] * t3 + kf[n + 1] * t2 + kf[n + 2] * t1 + kf[n + 3] * t0) * up(vr[n][cp]);
      if (l < 56) {
#pragma unroll
        for (int e = 0; e < 2; ++e)
#pragma unroll
          for (int h8 = 0; h8 < 2; ++h8) {
            u4 d = {pk(o[8 * h8 + 7][e], o[8 * h8 + 6][e]), pk(o[8 * h8 + 5][e], o[8 * h8 + 4][e]), pk(o[8 * h8 + 3][e], o[8 * h8 + 2][e]), pk(o[8 * h8 + 1][e], o[8 * h8][e])};
            lw128(l * 1024 + (2 * cp + e) * 128 + (((7 - 2 * w - h8) ^ (l & 7)) << 4), d);
          }
      }
    }
    u4 hd[2][4]; u2 hp[2][4];
#pragma unroll
    for (int r = 0; r < 2; ++r)
#pragma unroll
      for (int n = 0; n < 4; ++n) {
        hd[r][n] = ld128<0>(rH, l * 16 + n * 1024, (8 * sp + 2 * w + r) * 4096);
        hp[r][n] = ld64<0>(rH, (n == 0 && l == 0) ? 0x80000000u : l * 16 + n * 1024 - 8, (8 * sp + 2 * w + r) * 4096);
      }
    bar();
    STAMP(2);
#pragma unroll
    for (int n = 0; n < 14; ++n) {
      u4 d = lr128(n * 4096 + (tid >> 3) * 128 + (((tid & 7) ^ ((4 * n + w) & 7)) << 4));
      st128<SC1>(d, rW, (tid >> 3) * 4096 + (tid & 7) * 16 + WS_KV + n * 32 * 4096 + (31 - sp) * 128);
    }
    drain();
    bar();
    st64<SC1>(tag, rW, tid ? OOB : 0, WS_FK + sp * 8);
    STAMP(3);
    const u32 cbw = w * 33024, ub = 132096 + w * 4608;
#pragma unroll
    for (int r = 0; r < 2; ++r) {
#pragma unroll
      for (int n = 0; n < 4; ++n) put_copies(cbw + r * 16512 + 32 + n * 1024 + l * 16, hd[r][n], hp[r][n]);
    }
    if (l < 16) lw128(cbw + (l >> 1) * 4128 + (l & 1) * 16, (u4){0, 0, 0, 0});
    if (l < 32) lw128(ub + 4096 + l * 16, (u4){0, 0, 0, 0});
    wait_flags(rW, WS_FK + i32 * 8, tag);
    STAMP(4);
    u4 ud[2][4];
#pragma unroll
    for (int r = 0; r < 2; ++r)
#pragma unroll
      for (int n = 0; n < 4; ++n) ud[r][n] = ld128<SC1>(rW, l16 + n * 1024, WS_KV + (8 * sp + 2 * w + r) * 4096);
#pragma unroll
    for (int r = 0; r < 2; ++r) {
      const u32 ch = 8 * sp + 2 * w + r;
      const float dv = blo(((const unsigned short*)gD)[ch]);
      FENCE();
#pragma unroll
      for (int n = 0; n < 4; ++n) lw128(ub + n * 1024 + l * 16, ud[r][n]);
      FENCE();
      f4 acc[8];
#pragma unroll
      for (int n = 0; n < 8; ++n) {
        u2 uu = lr64(ub + 4088 - 512 * n - 32 * i16 - 8 * g4);
        acc[n] = (f4){bhi(uu[1]) * dv, blo(uu[1]) * dv, bhi(uu[0]) * dv, blo(uu[0]) * dv};
      }
#pragma unroll
      for (int q = 0; q < 4; ++q)
        conv_pair(acc, opq(cbw + r * 16512 + (i16 & 3) * 4128 + 8 + 16 * g4 + 8 * (i16 >> 2) + 128 * q), opq(ub + 480 - 32 * i16 + 16 * g4 + 128 * q));
#pragma unroll
      for (int n = 0; n < 8; ++n) st64<SC1>((u2){pk(acc[n][0], acc[n][1]), pk(acc[n][2], acc[n][3])}, rW, 32 * i16 + 8 * g4 + 512 * n, WS_Y + ch * 4096);
      drain();
      st64<SC1>(tag, rW, l ? OOB : 0, WS_FY + ch * 8);
      if (r) STAMP(6); else STAMP(5);
    }
  }
}
}

extern "C" void kernel_launch(void* const* d_in, const int* in_sizes, int n_in, void* d_out, int out_size, void* d_ws, size_t ws_size,
                              hipStream_t stream) {
  fk::fused<<<256, 256, 0, stream>>>(d_in[0], d_in[1], d_in[2], d_in[3], d_in[4], d_in[5], d_in[7], d_in[8], d_in[9], d_in[10], d_out, d_ws);
}
```

```cpp
#include <hip/hip_runtime.h>
#include <stdint.h>

namespace fk {
typedef unsigned u32;
typedef unsigned long long u64;
typedef __attribute__((ext_vector_type(8))) __bf16 bf8;
typedef __attribute__((ext_vector_type(2))) __bf16 bf2;
typedef __attribute__((ext_vector_type(4))) u32 u4;
typedef __attribute__((ext_vector_type(2))) u32 u2;
typedef __attribute__((ext_vector_type(2))) float f2;
typedef __attribute__((ext_vector_type(4))) float f4;
typedef __attribute__((ext_vector_type(16))) float f16v;
typedef __attribute__((ext_vector_type(4))) short s4;
typedef u4 __attribute__((aligned(8))) u4a8;
typedef __amdgpu_buffer_rsrc_t rsrc_t;
#define LDSP __attribute__((address_space(3)))
#define CSTP __attribute__((address_space(4)))
#define DEV __device__ __forceinline__
#define AI __attribute__((always_inline))
#define FENCE() asm volatile("" ::: "memory")
#define SB() __builtin_amdgcn_sched_barrier(0)
#define PINA(x) asm("" : "+a"(x))
#define CLR(a, b, c) st128<SC1>(a, b, c)
#define EXP_WR 1
#define KEEP(x) asm volatile("" : "+v"(x))
constexpr u32 OOB = 0x80000000u, SENT = 0xffffffffu;

constexpr u32 WS_E = 0;
constexpr u32 WS_F1 = 4096;
constexpr u32 WS_F2 = 8192;
constexpr u32 WS_ST = 65536;
constexpr u32 WS_Z = 1u << 20;
constexpr u32 WS_H = 5u << 20;
constexpr u32 WS_KV = 9u << 20;
constexpr u32 WS_Y = 11u << 20;
constexpr u32 WS_X = 13u << 20;
constexpr u32 WS_END = 21u << 20;
#define STAMP_OFF (32u << 20)
constexpr int SC1 = 16;
constexpr u32 SPIN = 1u << 17;
constexpr u32 CODE_BYTES = 54784;
constexpr int ZN = 16384;

__shared__ __attribute__((aligned(16))) char smem[163840];

template <int V> struct IC { static constexpr int v = V; };
template <int I, int N, class F> DEV void sfor(F&& f) {
  if constexpr (N - I == 1) f(IC<I>{});
  else if constexpr (N - I > 1) { sfor<I, (I + N) / 2>(f); sfor<(I + N) / 2, N>(f); }
}
DEV u32 opq(u32 x) { asm("" : "+v"(x)); return x; }
DEV u32 opqs(u32 x) { asm("" : "+s"(x)); return x; }
DEV LDSP char* lp(u32 off) { return (LDSP char*)smem + off; }
DEV u4 lr128(u32 off) { return *(LDSP u4*)lp(off); }
DEV u4 lr128a8(u32 off) { return *(LDSP u4a8*)lp(off); }
DEV u2 lr64(u32 off) { return *(LDSP u2*)lp(off); }
DEV void lw128(u32 off, u4 v) { *(LDSP u4*)lp(off) = v; }
DEV void lw64(u32 off, u2 v) { *(LDSP u2*)lp(off) = v; }
DEV void lw32(u32 off, u32 v) { *(LDSP u32*)lp(off) = v; }
DEV u2 ltr(u32 off) { return __builtin_bit_cast(u2, __builtin_amdgcn_ds_read_tr16_b64_v4i16((LDSP s4*)lp(off))); }
DEV u4 ltr2(u32 a, u32 b) { u2 x = ltr(a), y = ltr(b); return (u4){x[0], x[1], y[0], y[1]}; }
DEV void bar() { asm volatile("s_waitcnt lgkmcnt(0)\n\ts_barrier" ::: "memory"); }
template <int N> DEV void barn() { asm volatile("s_waitcnt lgkmcnt(%0)\n\ts_barrier" ::"i"(N) : "memory"); }
DEV void barv() { asm volatile("s_barrier" ::: "memory"); }
template <int N> DEV void vmw() { asm volatile("s_waitcnt vmcnt(%0)" ::"i"(N) : "memory"); }
template <int N> DEV void lgw() { asm volatile("s_waitcnt lgkmcnt(%0)" ::"i"(N) : "memory"); }
DEV rsrc_t mk(const void* p, u32 n) { return __builtin_amdgcn_make_buffer_rsrc((void*)p, 0, n, 0x00020000); }
DEV u4 mkd(const void* p, u32 n) { u64 a = (u64)p; return (u4){(u32)a, (u32)(a >> 32) & 0xffffu, n, 0x00020000u}; }
template <int LI, int SI, bool S1 = false> DEV void dma(u4 rs, u32 voff, u32 sb, u32 lb) {
  u32 t;
  if constexpr (S1)
    asm volatile("s_add_u32 m0, %4, %6\n\ts_add_u32 %0, %3, %5\n\tbuffer_load_dwordx4 %1, %2, %0 offen sc1 lds"
                 : "=&s"(t) : "v"(voff), "s"(rs), "s"(sb), "s"(lb), "i"(SI), "i"(LI) : "memory", "scc");
  else
    asm volatile("s_add_u32 m0, %4, %6\n\ts_add_u32 %0, %3, %5\n\tbuffer_load_dwordx4 %1, %2, %0 offen lds"
                 : "=&s"(t) : "v"(voff), "s"(rs), "s"(sb), "s"(lb), "i"(SI), "i"(LI) : "memory", "scc");
}
template <int AUX> DEV u4 ld128(rsrc_t r, u32 v, u32 s) { return __builtin_amdgcn_raw_buffer_load_b128(r, v, s, AUX); }
template <int AUX> DEV u2 ld64(rsrc_t r, u32 v, u32 s) { return __builtin_amdgcn_raw_buffer_load_b64(r, v, s, AUX); }
template <int AUX> DEV void st128(u4 d, rsrc_t r, u32 v) { __builtin_amdgcn_raw_buffer_store_b128(d, r, v, 0, AUX); }
template <int AUX> DEV void st64(u2 d, rsrc_t r, u32 v, u32 s) { __builtin_amdgcn_raw_buffer_store_b64(d, r, v, s, AUX); }
DEV f16v mm32(u4 a, u4 b, f16v c) { return __builtin_amdgcn_mfma_f32_32x32x16_bf16(__builtin_bit_cast(bf8, a), __builtin_bit_cast(bf8, b), c, 0, 0, 0); }
DEV f4 mm16(u4 a, u4 b, f4 c) { return __builtin_amdgcn_mfma_f32_16x16x32_bf16(__builtin_bit_cast(bf8, a), __builtin_bit_cast(bf8, b), c, 0, 0, 0); }
DEV u32 pk(float lo, float hi) { return __builtin_bit_cast(u32, __builtin_convertvector((f2){lo, hi}, bf2)); }
DEV float asf(u32 d) { return __builtin_bit_cast(float, d); }
DEV u32 asu(float d) { return __builtin_bit_cast(u32, d); }
DEV float blo(u32 d) { return asf(d << 16); }
DEV float bhi(u32 d) { return asf(d & 0xffff0000u); }
DEV float rbf(float x) { return blo(pk(x, x)); }
DEV f2 up(u32 d) { return (f2){blo(d), bhi(d)}; }
DEV u4 q4(const f16v& a, int n) { return __builtin_bit_cast(u4, (f4){a[4 * n], a[4 * n + 1], a[4 * n + 2], a[4 * n + 3]}); }
DEV float xhalf(float v) {
  auto r = __builtin_amdgcn_permlane32_swap(asu(v), asu(v), false, false);
  return asf(r[0]) + asf(r[1]);
}
DEV bool anyl(bool c) { return __builtin_amdgcn_ballot_w64(c) != 0; }
DEV u32 mx3(u32 a, u32 b, u32 c) { return max(max(a, b), c); }
DEV u32 mx4(u32 m, u4 v) { return mx3(mx3(m, v[0], v[1]), v[2], v[3]); }
DEV void wait_flags(u2 f, rsrc_t r, u32 voff, u2 tag) {
  if (__builtin_expect(anyl(f[0] != tag[0] || f[1] != tag[1]), 0)) {
#pragma nounroll
    for (u32 it = 0; it < SPIN; ++it) {
      FENCE();
      f = ld64<SC1>(r, voff, 0);
      if (!anyl(f[0] != tag[0] || f[1] != tag[1])) break;
      __builtin_amdgcn_s_sleep(2);
    }
  }
  FENCE();
}
DEV u2 next_tag(u64 e) {
  e = e * 6364136223846793005ull + 1442695040888963407ull;
  return (u2){(u32)e, (u32)(e >> 32)};
}

#define STAMP(k)

DEV f2 gelu2(f2 p, float ca, float cb, float inv) {
  f2 t = p * (p * p * cb + ca);
  f2 e = {__builtin_amdgcn_exp2f(t[0]), __builtin_amdgcn_exp2f(t[1])};
  f2 d = e * inv + inv;
  f2 q = {__builtin_amdgcn_rcpf(d[0]), __builtin_amdgcn_rcpf(d[1])};
  return p * q;
}

template <int NR, class F> DEV void conv_run(f4 (&acc)[8], u32 ha, u32 ua, F&& side) {
  u4 A[NR][8], B[NR][8];
#pragma unroll
  for (int a = 0; a < 8; ++a) { A[0][a] = lr128a8(ha + 512 * a); B[0][a] = lr128(ua + 512 * (7 - a)); }
  SB();
  sfor<0, NR * 36>([&](auto ii) AI {
    constexpr int r = decltype(ii)::v / 36, k = decltype(ii)::v % 36;
    constexpr int ta[36] = {0, 0, 1, 1, 0, 2, 1, 2, 2, 0, 3, 1, 3, 2, 3, 3, 0, 4, 1, 4, 2, 4, 3, 4, 0, 5, 1, 5, 2, 5, 0, 6, 1, 6, 0, 7};
    constexpr int tb[36] = {0, 1, 0, 1, 2, 0, 2, 1, 2, 3, 0, 3, 1, 3, 2, 3, 4, 0, 4, 1, 4, 2, 4, 3, 5, 0, 5, 1, 5, 2, 6, 0, 6, 1, 7, 0};
    if constexpr (r + 1 < NR && k >= 4 && (k & 1) == 0) {
      constexpr int f = (k - 4) >> 1, a = f >> 1;
      if constexpr (f & 1) B[r + 1][a] = lr128(ua + 512 * (7 - a) + 64 * (r + 1));
      else A[r + 1][a] = lr128a8(ha + 512 * a + 64 * (r + 1));
    }
    if constexpr ((k & 3) == 1) side(IC<r * 9 + (k >> 2)>{});
    acc[ta[k] + tb[k]] = mm16(A[r][ta[k]], B[r][tb[k]], acc[ta[k] + tb[k]]);
    SB();
  });
}
DEV void put_copies(u32 base, u4 d, u2 p) {
  u32 e0 = __builtin_amdgcn_alignbit(p[1], p[0], 16), e1 = __builtin_amdgcn_alignbit(d[0], p[1], 16);
  u32 e2 = __builtin_amdgcn_alignbit(d[1], d[0], 16), e3 = __builtin_amdgcn_alignbit(d[2], d[1], 16);
  u32 e4 = __builtin_amdgcn_alignbit(d[3], d[2], 16);
  lw128(base, (u4){e0, e1, e2, e3});
  lw128(base + 4128, (u4){p[1], d[0], d[1], d[2]});
  lw128(base + 8256, (u4){e1, e2, e3, e4});
  lw128(base + 12384, d);
}

__global__ __launch_bounds__(256, 1) void fused(const void* __restrict__ gx, const void* __restrict__ gwin, void* __restrict__ gws,
                                                const void* __restrict__ gwsp, const void* __restrict__ gk, const void* __restrict__ gv,
                                                const void* __restrict__ gsk, const void* __restrict__ gh, const void* __restrict__ gD,
                                                const void* __restrict__ gb, const void* __restrict__ gwo, void* __restrict__ gout) {
  STAMP(0);
  const u32 tid = threadIdx.x, l = tid & 63;
  {
    const u4 rC = mkd((const void*)(__builtin_amdgcn_s_getpc() & ~255ull), CODE_BYTES);
    asm volatile("s_mov_b32 m0, 0xf000\n\ts_nop 0\n\tbuffer_load_dword %0, %1, %2 offen lds" ::"v"(tid < 16 ? tid * 128 : OOB), "s"(rC), "s"((blockIdx.x >> 3) * 2048) : "memory");
  }
  const u32 w = __builtin_amdgcn_readfirstlane(tid >> 6);
  const u32 b = blockIdx.x, xcd = b & 7, j = b >> 3;
  const u32 i32 = l & 31, hh = l >> 5, i16 = l & 15, g4 = l >> 4;
  const u4 sent = {SENT, SENT, SENT, SENT};

  if (__builtin_expect(j < 28, 1)) {
    const u32 pl = j / 7, cl = j - pl * 7, p = 4 * (xcd >> 1) + pl, kap = xcd & 1, c = 2 * cl + kap;
    const u4 dX = mkd(gx, 2048 * 896), dWi = mkd(gwin, 448 * 3584);
    const u32 lw4 = w * 4096, lw1 = w * 1024, pw = p * 4 + w;
    const u32 sxa = ((l & 7) ^ (l >> 4)) << 4, sxw = ((l & 7) ^ (((l >> 4) & 1) << 2)) << 4;
    const u32 vx0 = (l >> 3) * 896 + sxa, vx1 = vx0 ^ 64, vw = (l >> 3) * 3584 + sxw;
    const u32 rotA = cl + 2 * pl - (cl + 2 * pl >= 7 ? 7 : 0);
    const u32 sx0 = pw * 32 * 896, sw0 = w * 8 * 3584 + c * 128;
    auto ka = [&](int kc) AI -> u32 { u32 t = kc + rotA; return t >= 7 ? t - 7 : t; };
    const rsrc_t rW = mk(gws, WS_END);
    const u4 dS = mkd(gwsp, 2048 * 4096), dO = mkd(gwo, 896 * 896);
    const u32 vs0 = (l >> 3) * 4096 + sxa, vs1 = vs0 ^ 64, vo = (l >> 3) * 896 + sxw;
    const u32 rotS = 2 * cl + 4 * pl, ss0 = pw * 32 * 4096 + kap * 2048;
    const u32 l16 = opq(l * 16);
    const u32 zo = l16 + (WS_Z + c * 262144 + pw * 2048), ho = l16 + (WS_H + pw * 57344 + c * 2048);
    const u32 xo = opq(l16 + (WS_X + b * 32768 + w * 8192));
    const u32 ch = 224 + xcd * 28 + j;
    constexpr int xz[7] = {4, 5, 6, 7, 8, 9, 4};
    constexpr int sz[16] = {9, 4, 0, 1, 2, 9, 4, 0, 1, 2, 9, 4, 8, 0, 1, 2};
    constexpr int oz[7] = {9, 3, 4, 8, 0, 1, 2};
    auto dmaS = [&](auto mm) AI {
      constexpr int m = decltype(mm)::v >> 2, r = decltype(mm)::v & 3;
      dma<sz[m] * ZN + r * 1024, r * 32768>(dS, (r & 1) ? vs1 : vs0, ss0 + ((m + rotS) & 15) * 128, lw4);
    };
    auto dmaO = [&](auto mm) AI {
      constexpr int m = decltype(mm)::v >> 2, r = decltype(mm)::v & 3;
      const u32 i = 7 * w + m;
      dma<oz[m] * ZN + r * 1024, ((r >> 1) * 32 + (r & 1) * 8) * 896>(dO, vo, ((i >> 1) * 64 + (i & 1) * 16) * 896 + cl * 128, lw4);
    };
    auto bg = [&](auto ii) AI {
      constexpr int i = decltype(ii)::v;
      if constexpr (i < 42) {
        constexpr int kc = i / 6, r = i % 6;
        if constexpr (r < 4) dma<xz[kc] * ZN + r * 1024, r * 7168>(dX, (r & 1) ? vx1 : vx0, sx0 + ka(kc) * 128, lw4);
        else dma<kc * 8192 + (r - 4) * 4096, (r - 4) * 32 * 3584 + 1792>(dWi, vw, sw0 + ka(kc) * (64 * 3584), lw1);
      } else if constexpr (i < 46) CLR(sent, rW, zo + ((i - 42) >> 1) * 131072 + (i & 1) * 1024);
      else if constexpr (i < 60) { constexpr int n = i - 46; dma<5 * ZN + n * 4096, (n & 1) * 32 * 3584>(dWi, vw, sw0 + ka(n >> 1) * (64 * 3584), lw1); }
      else if constexpr (i < 64) CLR(sent, rW, ho + ((i - 60) >> 1) * 28672 + (i & 1) * 1024);
      else if constexpr (i < 65) CLR(sent, rW, tid * 16 + (WS_Y + ch * 4096));
      else if constexpr (i < 69) dmaS(IC<i - 65>{});
      else if constexpr (i < 73) CLR(sent, rW, xo + (i - 69) * 1024);
      else if constexpr (i < 89) dmaS(IC<i - 69>{});
      else if constexpr (i < 93) CLR(sent, rW, xo + 4096 + (i - 89) * 1024);
      else dmaS(IC<i - 73>{});
      SB();
    };
    sfor<0, 24>(bg);
    const u64 e0 = *(const CSTP u64*)gws;
    const float dv = asf((((const CSTP u32*)gD)[ch >> 1] >> ((ch & 1) * 16)) << 16);
    STAMP(16);
    const u32 rdA = lw4 + i32 * 128 + ((hh ^ ((i32 >> 1) & 7)) << 4);
    u32 ra[4];
#pragma unroll
    for (int q = 0; q < 4; ++q) ra[q] = opq(4 * ZN + (rdA ^ ((q ^ w) << 5)));
    const u32 selv = ((g4 ^ i16) & 1) ? 0u : 0x3f803f80u;
    const u4 sel = {selv, selv, selv, selv};
    const u32 trA = (8 * hh + ((l >> 2) & 3)) * 128 + (((g4 & 1) * 32 + (l & 3) * 8) ^ (((l >> 3) & 1) << 6));
    u32 ta[4][2];
#pragma unroll
    for (int q = 0; q < 4; ++q) { ta[q][0] = opq(trA + ((q ^ w) << 11)); ta[q][1] = opq(ta[q][0] ^ 64); }
    const u2 tag = next_tag(e0);
    u4 xf[28];
    f16v z2[2], z1[2], gram = (f16v)0.f;
    f4 sxs = (f4)0.f, cs[2] = {(f4)0.f, (f4)0.f};
    z2[0] = (f16v)0.f; z2[1] = (f16v)0.f;
    float mean, rstd, sdev, ca, cb;
    u4 mop;
    auto cs_put = [&](u32 ex) AI {
      float cv = (g4 & 2) ? ((g4 & 1) ? cs[1][1] : cs[1][0]) : ((g4 & 1) ? cs[0][1] : cs[0][0]);
      lw32(ex + l * 16 + w * 4, asu(cv));
    };
    auto cs_get = [&](u32 ex, int n) AI -> u4 {
      f4 t = __builtin_bit_cast(f4, lr128(ex + (32 * n + i32) * 16));
      float s = (t[0] + t[1]) + (t[2] + t[3]);
      return (u4){pk(s, s - rbf(s)), 0, 0, 0};
    };
    vmw<18>();
#pragma unroll
    for (int q = 0; q < 4; ++q) xf[q] = lr128(ra[q]);
    barv();
    STAMP(17);
    sfor<0, 7>([&](auto kk) AI {
      constexpr int kc = decltype(kk)::v;
      constexpr int cum[8] = {24, 30, 36, 42, 50, 58, 65, 73};
      u4 wf[4][2];
      sfor<0, 4>([&](auto qq) AI {
        constexpr int q = decltype(qq)::v;
        wf[q][0] = ltr2(ta[q][0] + kc * 8192, ta[q][0] + kc * 8192 + 512);
        wf[q][1] = ltr2(ta[q][1] + kc * 8192, ta[q][1] + kc * 8192 + 512);
        gram = mm32(xf[4 * kc + q], xf[4 * kc + q], gram);
        SB();
        bg(IC<cum[kc] + q>{});
        sxs = mm16(sel, xf[4 * kc + q], sxs);
        SB();
      });
      u4 cop[2];
      sfor<0, 4>([&](auto qq) AI {
        constexpr int q = decltype(qq)::v;
        z2[0] = mm32(wf[q][0], xf[4 * kc + q], z2[0]);
        if constexpr (q == 0) cs[0] = mm16(sel, wf[0][0], cs[0]);
        if constexpr (kc < 6) SB();
        if constexpr (q < 2 && cum[kc] + 4 + 2 * q < cum[kc + 1]) bg(IC<cum[kc] + 4 + 2 * q>{});
        if constexpr (kc < 6 && q == 2) {
          vmw<cum[kc + 1] - 6 * (kc + 2)>();
#pragma unroll
          for (int r = 0; r < 4; ++r) xf[4 * kc + 4 + r] = lr128(ra[r] + (xz[kc + 1] - 4) * ZN);
        }
        if constexpr (kc == 6 && q == 1) {
          const bool b0 = i32 & 1, b1 = i32 & 2, b3 = i32 & 8, b4 = i32 & 16;
          float t[4];
#pragma unroll
          for (int r = 0; r < 4; ++r) t[r] = b1 ? (b0 ? gram[4 * r + 3] : gram[4 * r + 2]) : (b0 ? gram[4 * r + 1] : gram[4 * r]);
          float d = b4 ? (b3 ? t[3] : t[2]) : (b3 ? t[1] : t[0]);
          d = xhalf((((i32 >> 2) & 1) == hh) ? d : 0.f);
          const float m = (b4 ? sxs[1] : sxs[0]) * (1.f / 448.f);
          sdev = rbf(__builtin_amdgcn_sqrtf(rbf(rbf(fmaxf(d * (1.f / 448.f) - m * m, 0.f)) + 1e-5f)));
          rstd = __builtin_amdgcn_rcpf(sdev);
          const u32 mh = pk(-m, -m);
          mean = -blo(mh);
          mop = (u4){hh ? 0u : mh, 0, 0, 0};
          ca = -2.302208198f * rstd; cb = -0.1029432397f * rstd * rstd * rstd;
        }
        if constexpr (kc == 6 && q == 2) { cs_put(57344); }
        if constexpr (kc == 6 && q == 3) { cop[0] = cs_get(57344, 0); cop[1] = cs_get(57344, 1); }
        z2[1] = mm32(wf[q][1], xf[4 * kc + q], z2[1]);
        if constexpr (q == 0) cs[1] = mm16(sel, wf[0][1], cs[1]);
        SB();
        if constexpr (q < 2 && cum[kc] + 5 + 2 * q < cum[kc + 1]) bg(IC<cum[kc] + 5 + 2 * q>{});
        if constexpr (kc == 6 && q == 2) bar();
      });
      if constexpr (kc < 6) barv();
      else { z2[0] = mm32(cop[0], mop, z2[0]); z2[1] = mm32(cop[1], mop, z2[1]); }
      if constexpr (kc == 2) STAMP(18);
    });
    STAMP(1);
    {
      f2 a1 = {0.f, 0.f}, a2 = {0.f, 0.f};
      sfor<0, 8>([&](auto ii) AI {
        constexpr int n = decltype(ii)::v >> 2, r = (decltype(ii)::v & 3) * 4;
        bg(IC<73 + 2 * decltype(ii)::v>{});
        f2 u = gelu2((f2){z2[n][r], z2[n][r + 1]}, ca, cb, sdev), v = gelu2((f2){z2[n][r + 2], z2[n][r + 3]}, ca, cb, sdev);
        z2[n][r] = u[0]; z2[n][r + 1] = u[1]; z2[n][r + 2] = v[0]; z2[n][r + 3] = v[1];
        a1 += u + v; a2 += u * u; a2 += v * v;
        if constexpr (decltype(ii)::v < 7) bg(IC<74 + 2 * decltype(ii)::v>{});
      });
      float s1 = xhalf(a1[0] + a1[1]), s2 = xhalf(a2[0] + a2[1]);
      st128<SC1>((u4){asu(s1), tag[0], asu(s2), tag[1]}, rW, (hh ? OOB : i32 * 16) + (WS_ST + (pw * 14 + c) * 512));
      SB();
      bg(IC<88>{});
    }
    STAMP(2);
    vmw<30>();
    barv();
    st64<SC1>(tag, rW, tid ? OOB : 0, WS_F1 + b * 8);
    u4 Wf[64];
    u32 rs[4];
#pragma unroll
    for (int q = 0; q < 4; ++q) rs[q] = opq(rdA ^ (q << 5));
    auto rdS = [&](auto mm) AI { constexpr int m = decltype(mm)::v; Wf[m] = lr128(rs[m & 3] + sz[m >> 2] * ZN); };
    {
#pragma unroll
      for (int q = 0; q < 4; ++q) { ta[q][0] = opq(ta[q][0] + 5 * ZN); ta[q][1] = opq(ta[q][1] + 5 * ZN); }
      cs[0] = (f4)0.f; cs[1] = (f4)0.f;
      z1[0] = (f16v)0.f; z1[1] = (f16v)0.f;
      u4 wf[28][2];
      auto rd = [&](int s) AI {
        const u32 a = ta[s & 3][0] + (s >> 2) * 8192, a1 = ta[s & 3][1] + (s >> 2) * 8192;
        wf[s][0] = ltr2(a, a + 512); wf[s][1] = ltr2(a1, a1 + 512);
      };
      rd(0); rd(1); rd(2);
      SB();
      sfor<0, 28>([&](auto ss) AI {
        constexpr int s = decltype(ss)::v, g = s >> 2;
        constexpr int se[7] = {69, 77, 81, 85, 89, 99, 103};
        if constexpr (s + 3 < 28) rd(s + 3);
        z1[0] = mm32(wf[s][0], xf[s], z1[0]);
        if constexpr ((s & 3) == 0) cs[0] = mm16(sel, wf[s][0], cs[0]);
        SB();
        if constexpr ((s & 3) == 0) { vmw<91 + 4 * g - se[g] - (g == 4 ? 1 : 0)>(); if constexpr (g > 0) lgw<8>(); }
        bg(IC<89 + s>{});
        if constexpr ((s & 3) < 2) { rdS(IC<4 * g + 2 * (s & 1)>{}); rdS(IC<4 * g + 2 * (s & 1) + 1>{}); }
        if constexpr (s == 26) cs_put(58368);
        z1[1] = mm32(wf[s][1], xf[s], z1[1]);
        if constexpr ((s & 3) == 0) cs[1] = mm16(sel, wf[s][1], cs[1]);
        SB();
      });
    }
    STAMP(3);
    u32 z1p[16];
    u4 hd; u2 hp, f1; float bt;
    const u32 f1o = WS_F1 + (hh ? ((((i16 >> 1) & 3) * 7 + cl) * 8 + 4 * kap + 2 * (i16 >> 3) + (i16 & 1)) : 224 + i32) * 8;
    {
      const rsrc_t rH = mk(gh, 448 * 4096);
      u4 gq[7];
      auto ldg = [&]() AI {
#pragma unroll
        for (int n = 0; n < 7; ++n) gq[n] = ld128<SC1>(rW, hh * 3584 + i32 * 16 + n * 512, WS_ST + pw * 7168);
      };
      auto gbad = [&]() AI -> bool {
        u32 bad = 0;
#pragma unroll
        for (int n = 0; n < 7; ++n) bad |= (gq[n][1] ^ tag[0]) | (gq[n][3] ^ tag[1]);
        return anyl(opq(bad) != 0);
      };
      ldg();
      f1 = ld64<SC1>(rW, f1o, 0);
      hd = ld128<0>(rH, tid * 16, ch * 4096);
      hp = ld64<0>(rH, tid ? tid * 16 - 8 : OOB, ch * 4096);
      const u32 braw = ((const unsigned short*)gb)[32 * pw + i32];
      bar();
      z1[0] = mm32(cs_get(58368, 0), mop, z1[0]);
      z1[1] = mm32(cs_get(58368, 1), mop, z1[1]);
#pragma unroll
      for (int r = 0; r < 8; ++r) { f2 u = gelu2((f2){z1[0][2 * r], z1[0][2 * r + 1]}, ca, cb, sdev); z1p[r] = pk(u[0], u[1]); KEEP(z1p[r]); }
      STAMP(4);
      if (__builtin_expect(gbad(), 0)) {
#pragma nounroll
        for (u32 it = 0; it < SPIN; ++it) { FENCE(); ldg(); if (!gbad()) break; __builtin_amdgcn_s_sleep(1); }
      }
      STAMP(5);
      float s1 = 0.f, s2 = 0.f;
#pragma unroll
      for (int n = 0; n < 7; ++n) { s1 += asf(gq[n][0]); s2 += asf(gq[n][2]); }
      s1 = xhalf(s1) * (1.f / 896.f); s2 = xhalf(s2) * (1.f / 896.f);
      const float r2 = __builtin_amdgcn_rcpf(rbf(__builtin_amdgcn_sqrtf(rbf(rbf(fmaxf(s2 - s1 * s1, 0.f)) + 1e-5f)))), o2 = -rbf(s1) * r2;
      const u32 tb = 7 * ZN + lw4;
      const u32 sg = (i32 & 5) | ((i32 & 2) << 2) | ((i32 & 8) >> 2);
      const u32 twr = tb + i32 * 128 + ((hh ^ sg) << 3);
#pragma unroll
      for (int n = 0; n < 2; ++n)
#pragma unroll
        for (int rq = 0; rq < 4; ++rq) {
          f2 a = {z2[n][4 * rq], z2[n][4 * rq + 1]}, bq = {z2[n][4 * rq + 2], z2[n][4 * rq + 3]};
          a = a * r2 + o2; bq = bq * r2 + o2;
          lw64(twr ^ ((8 * n + 2 * rq) << 3), (u2){pk(a[0], a[1]), pk(bq[0], bq[1])});
        }
      FENCE();
      const u32 qq = (l >> 2) & 3;
      const u32 trd = tb + (8 * hh + qq) * 128 + (((4 * (g4 & 1) + (l & 3)) ^ ((qq & 1) | ((qq & 2) << 2) | (hh << 1))) << 3);
#pragma unroll
      for (int n = 0; n < 2; ++n)
#pragma unroll
        for (int q = 0; q < 2; ++q) {
          const u32 a = (trd ^ (n << 6)) + q * 2048;
          st128<SC1>(ltr2(a, (a ^ 32) + 512), rW, zo + n * 131072 + q * 1024);
        }
      bt = blo(braw);
      KEEP(bt);
    }
    STAMP(6);
    const u4 dW = mkd(gws, WS_END);
    u4 Zr[16][4];
    auto ldZ = [&](auto ss, int n) AI -> u4 {
      constexpr int s = decltype(ss)::v, sp = s >> 2, u = s & 3;
      return ld128<SC1>(rW, l16 + n * 1024, WS_Z + (2 * (sp < 2 ? c ^ 1 : c) + (sp & 1)) * 131072 + kap * 65536 + ((4 * u + w + rotS) & 15) * 4096);
    };
    {
      constexpr u32 cb0 = 5 * ZN, ub = cb0 + 16512;
      wait_flags(f1, rW, f1o, tag);
      dma<ub, WS_KV, true>(dW, l16, ch * 4096 + (3 - w) * 1024, (3 - w) * 1024);
      sfor<28, 44>(rdS);
      put_copies(cb0 + 32 + tid * 16, hd, hp);
      if (tid < 40) lw128(tid < 8 ? cb0 + (tid >> 1) * 4128 + (tid & 1) * 16 : ub + 4096 - 128 + tid * 16, (u4){0, 0, 0, 0});
      SB();
      sfor<0, 8>([&](auto ii) AI {
        constexpr int r = decltype(ii)::v;
        dmaS(IC<44 + r>{});
        f2 u = gelu2((f2){z1[1][2 * r], z1[1][2 * r + 1]}, ca, cb, sdev); z1p[8 + r] = pk(u[0], u[1]); KEEP(z1p[8 + r]);
      });
      vmw<8>();
      u4 ud = lr128(ub + (255 - tid) * 16);
      if (__builtin_expect(anyl(mx4(0, ud) == SENT), 0)) {
#pragma nounroll
        for (u32 it = 0; it < SPIN; ++it) {
          dma<ub, WS_KV, true>(dW, l16, ch * 4096 + (3 - w) * 1024, (3 - w) * 1024);
          vmw<0>();
          ud = lr128(ub + (255 - tid) * 16);
          if (!anyl(mx4(0, ud) == SENT)) break;
          __builtin_amdgcn_s_sleep(2);
        }
      }
      bar();
      STAMP(7);
      st64<SC1>(tag, rW, tid ? OOB : 0, WS_F2 + b * 8);
      f4 acc[8];
#pragma unroll
      for (int n = 0; n < 8; ++n) acc[n] = (f4)0.f;
      conv_run<2>(acc, opq(cb0 + (i16 & 3) * 4128 + 8 + 16 * g4 + 8 * (i16 >> 2) + 128 * w), opq(ub + 480 - 32 * i16 + 16 * g4 + 128 * w), [&](auto kk) AI {
        constexpr int k = decltype(kk)::v;
        if constexpr (k < 12) dmaS(IC<52 + k>{});
        else dmaO(IC<k - 12>{});
      });
      dmaO(IC<6>{}); dmaO(IC<7>{});
      vmw<20>();
      sfor<44, 52>(rdS);
      bar();
      STAMP(24);
#pragma unroll
      for (int n = 0; n < 8; ++n) lw128(cb0 + w * 8192 + n * 1024 + i16 * 64 + ((g4 ^ ((i16 >> 1) & 3)) << 4), __builtin_bit_cast(u4, acc[n]));
      sfor<8, 16>(dmaO);
      vmw<16>();
      sfor<52, 64>(rdS);
      SB();
      sfor<0, 4>([&](auto ss) AI {
#pragma unroll
        for (int n = 0; n < 4; ++n) { Zr[decltype(ss)::v][n] = ldZ(ss, n); SB(); }
      });
      bar();
      f4 y0 = (f4)0.f, y1 = (f4)0.f;
      const u32 ry = cb0 + ((2 * tid) ^ ((tid >> 2) & 3)) * 16;
#pragma unroll
      for (int n = 0; n < 4; ++n) { y0 += __builtin_bit_cast(f4, lr128(ry + n * 8192)); y1 += __builtin_bit_cast(f4, lr128((ry ^ 16) + n * 8192)); }
      u4 o;
      o[0] = pk(y0[0] + bhi(ud[3]) * dv, y0[1] + blo(ud[3]) * dv); o[1] = pk(y0[2] + bhi(ud[2]) * dv, y0[3] + blo(ud[2]) * dv);
      o[2] = pk(y1[0] + bhi(ud[1]) * dv, y1[1] + blo(ud[1]) * dv); o[3] = pk(y1[2] + bhi(ud[0]) * dv, y1[3] + blo(ud[0]) * dv);
      st128<SC1>(o, rW, tid * 16 + (WS_Y + ch * 4096));
    }
    STAMP(8);
    {
      auto zbad = [&](auto ss) AI -> bool {
        constexpr int s = decltype(ss)::v;
        return anyl(mx4(mx4(mx4(mx4(0, Zr[s][0]), Zr[s][1]), Zr[s][2]), Zr[s][3]) == SENT);
      };
      auto zfix = [&](auto ss) AI {
        if (__builtin_expect(zbad(ss), 0)) {
#pragma nounroll
          for (u32 it = 0; it < SPIN; ++it) {
            FENCE();
#pragma unroll
            for (int n = 0; n < 4; ++n) Zr[decltype(ss)::v][n] = ldZ(ss, n);
            if (!zbad(ss)) break;
            __builtin_amdgcn_s_sleep(2);
          }
        }
      };
      constexpr int slot[3] = {7 * ZN, 5 * ZN, 6 * ZN};
      const u32 zrd = opq(l16 + 5 * ZN), zwr = opq(zrd + lw4);
      zfix(IC<0>{});
      STAMP(9);
#pragma unroll
      for (int n = 0; n < 4; ++n) lw128(zwr + (slot[0] - 5 * ZN) + n * 1024, Zr[0][n]);
      bar();
      u4 zf[256];
#pragma unroll
      for (int n = 0; n < 6; ++n) zf[n] = lr128(zrd + (slot[0] - 5 * ZN) + n * 1024);
      f16v acc[4];
      u4 pp[2][4];
      u2 f2v;
      f2 hu[8], hv[8];
      const u32 xi = xo ^ 32768;
      auto ppbad = [&](int n) AI -> bool { return anyl(mx4(mx4(mx4(mx4(0, pp[n][0]), pp[n][1]), pp[n][2]), pp[n][3]) == SENT); };
      auto ppfix = [&](int n) AI {
        if (__builtin_expect(ppbad(n), 0)) {
#pragma nounroll
          for (u32 it = 0; it < SPIN; ++it) {
            FENCE();
#pragma unroll
            for (int r = 0; r < 4; ++r) pp[n][r] = ld128<SC1>(rW, xi + n * 4096 + r * 1024, 0);
            if (!ppbad(n)) break;
            __builtin_amdgcn_s_sleep(2);
          }
        }
      };
      auto hpre = [&](int n, int r) AI {
        hu[r] = up(z1p[8 * n + r]);
        hv[r] = ((f2){asf(pp[n][r >> 1][2 * (r & 1)]), asf(pp[n][r >> 1][2 * (r & 1) + 1])} + bt) * hu[r];
        asm volatile("" : "+v"(hu[r]), "+v"(hv[r]));
      };
      auto hout = [&](int n, int q) AI {
        u4 o;
#pragma unroll
        for (int d = 0; d < 4; ++d) { f2 t = (f2){acc[2 + n][8 * q + 2 * d], acc[2 + n][8 * q + 2 * d + 1]} * hu[4 * q + d] + hv[4 * q + d]; o[d] = pk(t[0], t[1]); }
        st128<SC1>(o, rW, ho + n * 28672 + q * 1024);
      };
      SB();
      sfor<0, 256>([&](auto ii) AI {
        constexpr int i = decltype(ii)::v, s = i >> 4, g = i & 15, sp = s >> 2;
        auto rz = [&](auto ff) AI { constexpr int f = decltype(ff)::v; if constexpr (f < 256) zf[f] = lr128(zrd + (slot[(f >> 4) % 3] - 5 * ZN) + (f & 15) * 1024); };
        if constexpr (g == 0 && s < 15) zfix(IC<s + 1>{});
        if constexpr (g == 0 && (s == 11 || s == 14)) ppfix(s == 14);
        if constexpr (g == 0 && s == 7) wait_flags(f2v, rW, WS_F2 + tid * 8, tag);
        if constexpr (g == 10 && s < 15) barn<2>();
        if constexpr (g < 8) {
          if constexpr (g & 1) { if constexpr (s < 15 && EXP_WR) lw128(zwr + (slot[(s + 1) % 3] - 5 * ZN) + (g >> 1) * 1024, Zr[s + 1][g >> 1]); }
          else { rz(IC<i + 6>{}); rz(IC<i + 7>{}); }
        } else {
          rz(IC<i + 6>{});
          if constexpr ((g & 1) == 0 && s + 4 < 16) Zr[s + 4][(g - 8) >> 1] = ldZ(IC<s + 4>{}, (g - 8) >> 1);
          if constexpr ((g & 1) == 1) {
            constexpr int r = (g - 8) >> 1;
            if constexpr (s == 4 || s == 8) st128<SC1>(q4(acc[sp - 1], r), rW, xo + (sp - 1) * 4096 + r * 1024);
            if constexpr (s == 9 || s == 12) pp[s == 12][r] = ld128<SC1>(rW, xi + (s == 12) * 4096 + r * 1024, 0);
            if constexpr (s == 5 && r == 0) f2v = ld64<SC1>(rW, WS_F2 + tid * 8, 0);
          }
          if constexpr (s == 11) hpre(0, g - 8);
          if constexpr (s == 15) hpre(1, g - 8);
        }
        if constexpr (s == 12 && (g == 3 || g == 7)) hout(0, g >> 2);
        if constexpr (s >= 14 && (i - 224) % 2 == 1 && i - 224 < 24) dmaO(IC<16 + ((i - 224) >> 1)>{});
        if constexpr (i < 52) PINA(Wf[i]);
        if constexpr ((i & 63) == 0) acc[sp] = (f16v)0.f;
        acc[sp] = mm32(zf[i], Wf[i & 63], acc[sp]);
        SB();
        if constexpr ((i & 63) == 0) STAMP(32 + sp);
      });
      hout(1, 0); hout(1, 1);
    }
    STAMP(10);
    {
      const rsrc_t rX = mk(gx, 2048 * 896);
      const u32 T0 = 128 * p + 64 * kap + 32 * (w >> 1), D0 = 64 * cl + 32 * (w & 1);
      u4 Hf[2][2][7];
      auto ldH = [&](int n) AI {
#pragma unroll
        for (int t = 1; t >= 0; --t)
#pragma unroll
          for (int m = 0; m < 7; ++m) { Hf[n][t][m] = ld128<SC1>(rW, l16 + (m & 3) * 1024, WS_H + (p * 4 + 2 * kap + (t ^ (w >> 1))) * 57344 + n * 28672 + (7 * w + (m & 4)) * 1024); SB(); }
      };
      auto hbad = [&](int n) AI -> bool {
        u32 m0 = 0, m1 = 0;
#pragma unroll
        for (int m = 0; m < 7; ++m) { m0 = mx4(m0, Hf[n][0][m]); m1 = mx4(m1, Hf[n][1][m]); }
        return anyl(max(m0, m1) == SENT);
      };
      auto hfix = [&](int n) AI {
        if (__builtin_expect(hbad(n), 0)) {
#pragma nounroll
          for (u32 it = 0; it < SPIN; ++it) { FENCE(); ldH(n); if (!hbad(n)) break; __builtin_amdgcn_s_sleep(2); }
        }
      };
      u4 yr[2];
      auto ldY = [&]() AI {
#pragma unroll
        for (int n = 0; n < 2; ++n) yr[n] = ld128<SC1>(rW, (l >> 2) * 4096 + (l & 3) * 16, WS_Y + (D0 + 16 * n) * 4096 + T0 * 2);
      };
      ldH(0);
      ldY();
      u2 xv[4];
#pragma unroll
      for (int r = 0; r < 4; ++r) xv[r] = ld64<0>(rX, i32 * 896 + hh * 8 + r * 16, T0 * 896 + D0 * 2);
      lw64(5 * ZN + (32 * w + i32) * 8, (u2){asu(mean), asu(rstd)});
      const u32 tro = opq(lw4 + (4 * hh + ((l >> 2) & 3)) * 128 + (((g4 & 1) * 32 + (l & 3) * 8) ^ ((((l >> 3) ^ w) & 1) << 6))), tro1 = opq(tro ^ 64);
      u4 Af[2][7][2];
      vmw<22>();
#pragma unroll
      for (int n = 0; n < 2; ++n)
#pragma unroll
        for (int m = 0; m < 7; ++m) {
          const u32 o = oz[m] * ZN + n * 2048;
          Af[n][m][0] = ltr2(tro + o, tro + o + 1024); Af[n][m][1] = ltr2(tro1 + o, tro1 + o + 1024);
        }
      bar();
      STAMP(11);
      hfix(0);
      f16v o[4];
#pragma unroll
      for (int lam = 3; lam >= 0; --lam) {
        o[lam] = (f16v)0.f;
#pragma unroll
        for (int m = 0; m < 7; ++m) { if (lam >= 2) PINA(Af[0][m][lam & 1]); o[lam] = mm32(Af[0][m][lam & 1], Hf[0][lam >> 1][m], o[lam]); }
      }
      SB();
      ldH(1);
      if (__builtin_expect(anyl(mx4(mx4(0, yr[0]), yr[1]) == SENT), 0)) {
#pragma nounroll
        for (u32 it = 0; it < SPIN; ++it) { FENCE(); ldY(); if (!anyl(mx4(mx4(0, yr[0]), yr[1]) == SENT)) break; __builtin_amdgcn_s_sleep(2); }
      }
      const u32 yb = 6 * ZN + w * 2048;
#pragma unroll
      for (int n = 0; n < 2; ++n) lw128(yb + n * 1024 + l16, yr[n]);
      FENCE();
      const u2 ms = lr64(5 * ZN + (64 * kap + 32 * (w >> 1) + i32) * 8);
      const float rsd = asf(ms[1]), mo = -asf(ms[0]) * rsd;
      f2 sv[8];
#pragma unroll
      for (int r = 0; r < 4; ++r) {
        u2 yy = ltr(yb + (8 * r + 4 * hh + ((l >> 2) & 3)) * 64 + (g4 & 1) * 32 + (l & 3) * 8);
        sv[2 * r] = (up(xv[r][0]) * rsd + mo) * up(yy[0]);
        sv[2 * r + 1] = (up(xv[r][1]) * rsd + mo) * up(yy[1]);
        asm volatile("" : "+v"(sv[2 * r]), "+v"(sv[2 * r + 1]));
      }
      STAMP(12);
      hfix(1);
      STAMP(13);
      const u32 ew = opq(l16 + w * 16384);
      sfor<0, 28>([&](auto ii) AI {
        constexpr int lam = 3 - decltype(ii)::v / 7, m = decltype(ii)::v % 7;
        if constexpr (lam >= 2) PINA(Af[1][m][lam & 1]);
        o[lam] = mm32(Af[1][m][lam & 1], Hf[1][lam >> 1][m], o[lam]);
        if constexpr (lam < 3 && m >= 2 && m < 6) lw128(ew + (lam + 1) * 4096 + (m - 2) * 1024, q4(o[lam + 1], m - 2));
        SB();
      });
      bar();
#pragma unroll
      for (int lam = 1; lam < 4; ++lam)
#pragma unroll
        for (int n = 0; n < 4; ++n) {
          f4 v = __builtin_bit_cast(f4, lr128((ew ^ (lam * 16384)) + lam * 4096 + n * 1024));
#pragma unroll
          for (int r = 0; r < 4; ++r) o[0][4 * n + r] += v[r];
        }
      const rsrc_t rOut = mk(gout, 2048 * 896);
#pragma unroll
      for (int r = 0; r < 4; ++r) {
        u2 ov;
#pragma unroll
        for (int d = 0; d < 2; ++d) {
          f2 v = {o[0][4 * r + 2 * d], o[0][4 * r + 2 * d + 1]};
          f2 t = v * -1.442695041f;
          f2 e = {__builtin_amdgcn_exp2f(t[0]), __builtin_amdgcn_exp2f(t[1])};
          e = e + 1.f;
          f2 q = {__builtin_amdgcn_rcpf(e[0]), __builtin_amdgcn_rcpf(e[1])};
          v = v * q * sv[2 * r + d] + up(xv[r][d]);
          ov[d] = pk(v[0], v[1]);
        }
        st64<0>(ov, rOut, i32 * 896 + hh * 8 + r * 16, T0 * 896 + D0 * 2);
      }
    }
    if (b == 0) st64<SC1>(tag, rW, tid ? OOB : 0, WS_E);
  } else {
    const u32 sp = b - 224;
    const rsrc_t rW = mk(gws, WS_END), rH = mk(gh, 448 * 4096);
    const rsrc_t rK = mk(gk, 2048 * 896), rV = mk(gv, 2048 * 896), rSk = mk(gsk, 448 * 8);
    const u32 R0 = 64 * sp + 16 * w;
    const u32 l16 = opq(l * 16);
    const u32 lv = opq(l < 56 ? l16 : OOB);
    u4 kr[19], vr[16], sk[4];
#pragma unroll
    for (int n = 0; n < 4; ++n) sk[n] = ld128<0>(rSk, l < 56 ? l * 64 + n * 16 : OOB, 0);
#pragma unroll
    for (int n = 0; n < 3; ++n) kr[n] = ld128<0>(rK, R0 ? lv + n * 896 : OOB, R0 ? (R0 - 3) * 896 : 0);
#pragma unroll
    for (int n = 0; n < 16; ++n) { kr[n + 3] = ld128<0>(rK, lv + (n & 3) * 896, (R0 + (n & 12)) * 896); vr[n] = ld128<0>(rV, lv + (n & 3) * 896, (R0 + (n & 12)) * 896); SB(); }
    const u2 tag = next_tag(*(const CSTP u64*)gws);
    const u32 ko = (tid >> 3) * 4096 + (tid & 7) * 16 + (WS_KV + (31 - sp) * 128);
    const u32 yo = opq(tid * 16 + (WS_Y + sp * 7 * 4096));
    STAMP(1);
    sfor<0, 8>([&](auto ii) AI {
      constexpr int h8 = decltype(ii)::v >> 2, cp = decltype(ii)::v & 3;
      const f2 t0 = {blo(sk[cp][0]), blo(sk[cp][2])}, t1 = {bhi(sk[cp][0]), bhi(sk[cp][2])};
      const f2 t2 = {blo(sk[cp][1]), blo(sk[cp][3])}, t3 = {bhi(sk[cp][1]), bhi(sk[cp][3])};
      f2 kf[11], o[8];
#pragma unroll
      for (int n = 0; n < 11; ++n) kf[n] = up(kr[8 * h8 + n][cp]);
#pragma unroll
      for (int n = 0; n < 8; ++n) o[n] = (kf[n] * t3 + kf[n + 1] * t2 + kf[n + 2] * t1 + kf[n + 3] * t0) * up(vr[8 * h8 + n][cp]);
#pragma unroll
      for (int e = 0; e < 2; ++e) {
        u4 d = {pk(o[7][e], o[6][e]), pk(o[5][e], o[4][e]), pk(o[3][e], o[2][e]), pk(o[1][e], o[0][e])};
        lw128(l * 1024 + (2 * cp + e) * 128 + (((7 - 2 * w - h8) ^ (l & 7)) << 4), d);
      }
      if constexpr (h8 == 0) {
#pragma unroll
        for (int n = 0; n < 6; ++n) {
          constexpr int z = 0;
          const int k = cp * 6 + n;
          if (k < 14) CLR(sent, rW, ko + k * 131072);
          else if (k < 21) CLR(sent, rW, yo + (k - 14) * 4096);
        }
      }
      SB();
    });
    u4 hd[2][4]; u2 hp[2][4];
#pragma unroll
    for (int r = 0; r < 2; ++r)
#pragma unroll
      for (int n = 0; n < 4; ++n) {
        hd[r][n] = ld128<0>(rH, l16 + n * 1024, (7 * sp + w + 4 * r) * 4096);
        hp[r][n] = ld64<0>(rH, (n == 0 && l == 0) ? OOB : l16 + n * 1024 - 8, (7 * sp + w + 4 * r) * 4096);
      }
    SB();
    vmw<16>();
    bar();
    STAMP(2);
#pragma unroll
    for (int n = 0; n < 14; ++n) {
      u4 d = lr128(n * 4096 + (tid >> 3) * 128 + (((tid & 7) ^ ((4 * n + w) & 7)) << 4));
      st128<SC1>(d, rW, ko + n * 131072);
    }
    st64<SC1>(tag, rW, tid > 1 ? OOB : tid * 4096, WS_F1 + b * 8);
    bar();
    STAMP(3);
    const u32 cbw = w * 33024, ub = 132096 + w * 4608;
#pragma unroll
    for (int r = 0; r < 2; ++r) {
#pragma unroll
      for (int n = 0; n < 4; ++n) put_copies(cbw + r * 16512 + 32 + n * 1024 + l16, hd[r][n], hp[r][n]);
    }
    if (l < 48) lw128(l < 16 ? cbw + (l >> 1) * 4128 + (l & 1) * 16 : ub + 4096 - 256 + l16, (u4){0, 0, 0, 0});
    wait_flags((u2){~tag[0], 0}, rW, WS_F1 + (224 + i32) * 8, tag);
    STAMP(4);
#pragma nounroll
    for (u32 r = 0; r < 2; ++r) {
      const u32 ci = w + 4 * r;
      if (ci >= 7) break;
      const u32 ch = 7 * sp + ci;
      const float dv = asf((((const CSTP u32*)gD)[ch >> 1] >> ((ch & 1) * 16)) << 16);
      u4 ud[4];
#pragma nounroll
      for (u32 it = 0; it < SPIN; ++it) {
        FENCE();
#pragma unroll
        for (int n = 0; n < 4; ++n) ud[n] = ld128<SC1>(rW, l16 + n * 1024, WS_KV + ch * 4096);
        if (!anyl(mx4(mx4(mx4(mx4(0, ud[0]), ud[1]), ud[2]), ud[3]) == SENT)) break;
        __builtin_amdgcn_s_sleep(2);
      }
#pragma unroll
      for (int n = 0; n < 4; ++n) lw128(ub + n * 1024 + l16, ud[n]);
      FENCE();
      f4 acc[8];
#pragma unroll
      for (int n = 0; n < 8; ++n) {
        u2 uu = lr64(ub + 4088 - 512 * n - 32 * i16 - 8 * g4);
        acc[n] = (f4){bhi(uu[1]) * dv, blo(uu[1]) * dv, bhi(uu[0]) * dv, blo(uu[0]) * dv};
      }
      conv_run<8>(acc, opq(cbw + r * 16512 + (i16 & 3) * 4128 + 8 + 16 * g4 + 8 * (i16 >> 2)), opq(ub + 480 - 32 * i16 + 16 * g4), [](auto) AI {});
#pragma unroll
      for (int n = 0; n < 8; ++n) st64<SC1>((u2){pk(acc[n][0], acc[n][1]), pk(acc[n][2], acc[n][3])}, rW, 32 * i16 + 8 * g4 + 512 * n, WS_Y + ch * 4096);
      FENCE();
      if (r == 0) STAMP(5);
    }
  }
}
}

extern "C" void kernel_launch(void* const* d_in, const int* in_sizes, int n_in, void* d_out, int out_size, void* d_ws, size_t ws_size,
                              hipStream_t stream) {
  fk::fused<<<256, 256, 0, stream>>>(d_in[0], d_in[7], d_ws, d_in[8], d_in[1], d_in[2], d_in[3], d_in[4], d_in[5], d_in[9], d_in[10], d_out);
}
```

```cpp
#include <hip/hip_runtime.h>
#include <stdint.h>

namespace fk {
typedef unsigned u32;
typedef unsigned long long u64;
typedef __attribute__((ext_vector_type(8))) __bf16 bf8;
typedef __attribute__((ext_vector_type(2))) __bf16 bf2;
typedef __attribute__((ext_vector_type(16))) u32 u16v;
typedef __attribute__((ext_vector_type(4))) u32 u4;
typedef __attribute__((ext_vector_type(2))) u32 u2;
typedef __attribute__((ext_vector_type(2))) float f2;
typedef __attribute__((ext_vector_type(4))) float f4;
typedef __attribute__((ext_vector_type(16))) float f16v;
typedef __attribute__((ext_vector_type(4))) short s4;
typedef u4 __attribute__((aligned(8))) u4a8;
typedef __amdgpu_buffer_rsrc_t rsrc_t;
#define LDSP __attribute__((address_space(3)))
#define DEV __device__ __forceinline__
#define AI __attribute__((always_inline))
#define FENCE() asm volatile("" ::: "memory")
#define SB() __builtin_amdgcn_sched_barrier(0)
#define COLD() asm volatile("; @C" ::: "memory")
constexpr u32 OOB = 0x80000000u, SENT = 0xffffffffu;

#define PFS 8
#define PFH 2
#define AROT 1
#define BW 5
#define NB 16
#define CD 0
#define SG 7
#define FS 9
#define HS 16
#define SE 48
#define RE 36
#define ZK 4
#define PZ 4
#define CAP 5
#define OP 0
#define G0 0
#define E0 160
#define E1 187
#define E2 212
#define E3 247
#define FYL 234
#define A0 209
#define H0AT 236
#define H1AT 12
#define X1 165
#define XG1 6
#define HN 6
#define HG 2
#define PF1 8
#define KJ 5
#define NK 4
#define CM 0xfffffff
#define RM 0x2aaaaaaabbeull
#define F3L 159
#define F3C 185
#define DS 20
#define F1S 21
#define F1C 9
#define FCS 2
#define FCC 18
#define F2K 2
#define ND 3
#define HCK 8
#define HCS 4
#define HCF 200
#define HCM 1
#define ZF 0
#define FL 16
#define PFG 2
#define YLATE 0
#define XS 3072
#define YAT 6
#define XG 4
#define F2L 0
#define F2C 19
#define DL 2
#define GE 0
#define OM 0xfff8fffcffffffffull
constexpr int RA = 8, NCHK = 5;

constexpr u32 WS_E = 0;
constexpr u32 WS_F1 = 655360;
constexpr u32 WS_F2 = 786432;
constexpr u32 WS_FZ = 917504;
constexpr u32 WS_FH = 933888;
constexpr u32 WS_FY = 4096;
constexpr u32 WS_ST = 65536;
constexpr u32 WS_Z = 1u << 20;
constexpr u32 WS_H = 5u << 20;
constexpr u32 WS_KV = 9u << 20;
constexpr u32 WS_Y = 11u << 20;
constexpr u32 WS_X = 13u << 20;
constexpr u32 WS_END = 21u << 20;
#define STAMP_OFF (32u << 20)
constexpr int SC1 = 16;
constexpr u32 SPIN = 1u << 17;
constexpr u32 CODE_BYTES = 57088;
constexpr int ZN = 16384;
constexpr int WT[] = { 12, 10, 10, 10, 10, 10, 12, 12, 10, 0, 20, 18, 17, 17, 16, 14, 23, 19, 15, 35, 34, 38, 17, 18, 18, 17, 9, 6, 0, 11, 7, 9, 10, 14, 17, 21, 21, 0, 21, 19, 19, 20, 23, 20, 17, 13, 13, 11, 16, 6, 14, 12, 10, 8, 8, 8, 8, 12, 10, 8, 6, 4, 2, 0, 14, 20, 6, 0, 2, 17, 0, 0, 0, 0, 0, 0, 0, 0, 0, 0, 0, 0, 0, 0, 15, 0, 0, 0, 0, 0, 0, 0, 0, 0, 0, 0 };
enum { MA = 0, MW1 = 7, MZC = 8, MZS = 9, MS = 10, MG = 26, MF1 = 27, MH = 28, MKV = 29, MZ = 30, MEX = 46, MFY = 48, MF2 = 49, MH0 = 50, MXY = 64, MYS = 65, MSH = 66, MSU = 67, MK = 68, MF3 = 69, MO = 70, MF4 = 84 };

__shared__ __attribute__((aligned(16))) char smem[163840];

template <int V> struct IC { static constexpr int v = V; };
template <int I, int N, class F> DEV void sfor(F&& f) {
  if constexpr (N - I == 1) f(IC<I>{});
  else if constexpr (N - I > 1) { sfor<I, (I + N) / 2>(f); sfor<(I + N) / 2, N>(f); }
}
template <class T> DEV void keep1(T& x) { asm volatile("" : "+v"(x)); }
template <class... T> DEV void KEEP(T&... x) { (keep1(x), ...); }
DEV void PINA(u4& x) { asm("" : "+a"(x)); }
DEV void ties(u64& a, u32& b) { asm volatile("" : "+s"(a), "+s"(b) :: "memory"); }
DEV u32 opq(u32 x) { asm("" : "+v"(x)); return x; }
DEV float opf(float x) { asm("" : "+v"(x)); return x; }
DEV u32 opqs(u32 x) { asm("" : "+s"(x)); return x; }
DEV LDSP char* lp(u32 off) { return (LDSP char*)smem + off; }
DEV u4 lr128(u32 off) { return *(LDSP u4*)lp(off); }
DEV u4 lr128a8(u32 off) { return *(LDSP u4a8*)lp(off); }
DEV u32 lr32(u32 off) { return *(LDSP u32*)lp(off); }
DEV u2 lr64(u32 off) { return *(LDSP u2*)lp(off); }
DEV void lw128(u32 off, u4 v) { *(LDSP u4*)lp(off) = v; }
DEV void lw64(u32 off, u2 v) { *(LDSP u2*)lp(off) = v; }
DEV void lw32(u32 off, u32 v) { *(LDSP u32*)lp(off) = v; }
DEV u2 ltr(u32 off) { return __builtin_bit_cast(u2, __builtin_amdgcn_ds_read_tr16_b64_v4i16((LDSP s4*)lp(off))); }
DEV u4 ltr2(u32 a, u32 b) { u2 x = ltr(a), y = ltr(b); return (u4){x[0], x[1], y[0], y[1]}; }
DEV void barv() { asm volatile("s_barrier" ::: "memory"); }
template <int N> DEV void lgw() { FENCE(); __builtin_amdgcn_s_waitcnt(0xc07f | (N << 8)); FENCE(); }
DEV void bar() { lgw<0>(); barv(); }
template <int N> DEV void vmw() { asm volatile("s_waitcnt vmcnt(%0)" ::"i"(N) : "memory"); }
template <int M> DEV void mark() { asm volatile("; @M %0" ::"i"(M) : "memory"); }
template <int M> DEV void vw() { asm volatile("s_waitcnt vmcnt(%0) ; @W %1" ::"i"(WT[M]), "i"(M) : "memory"); }
template <int M> DEV void vneed() { asm volatile("; @VN %0" ::"i"(M) : "memory"); }
template <int M> DEV void lmark() { asm volatile("; @LM %0" ::"i"(M) : "memory"); }
template <int M> DEV void lneed() { asm volatile("; @LN %0" ::"i"(M) : "memory"); }
DEV rsrc_t mk(u4 d) { return __builtin_amdgcn_make_buffer_rsrc((void*)(((u64)d[1] << 32) | d[0]), 0, d[2], 0x00020000); }
template <int LI, int SI, int OFF = 0> DEV u32 dma1(u4 rs, u32 voff, u32 sb, u32 lb) {
  u32 t;
  asm volatile("s_add_u32 m0, %4, %6 ; @m0\n\ts_add_u32 %0, %3, %5\n\tbuffer_load_dwordx4 %1, %2, %0 offen offset:%7 lds"
               : "=&s"(t) : "v"(voff), "s"(rs), "s"(sb), "s"(lb), "i"(SI), "i"(LI), "i"(OFF) : "memory", "scc");
  return t;
}
template <int LI, int SI, int OFF = 0> DEV u32 dmas(u4 rs, u32 voff, u32 sb, u32 lb) {
  u32 t;
  asm volatile("s_add_u32 m0, %4, %6 ; @m0\n\ts_add_u32 %0, %3, %5\n\tbuffer_load_dwordx4 %1, %2, %0 offen offset:%7 sc1 lds"
               : "=&s"(t) : "v"(voff), "s"(rs), "s"(sb), "s"(lb), "i"(SI), "i"(LI), "i"(OFF) : "memory", "scc");
  return t;
}
template <int OFF> DEV void dmasn(u4 rs, u32 voff, u32 so) {
  asm volatile("buffer_load_dwordx4 %0, %1, %2 offen offset:%3 sc1 lds" ::"v"(voff), "s"(rs), "s"(so), "i"(OFF) : "memory");
}
template <int OFF> DEV void dman(u4 rs, u32 voff, u32 so) {
  asm volatile("buffer_load_dwordx4 %0, %1, %2 offen offset:%3 lds" ::"v"(voff), "s"(rs), "s"(so), "i"(OFF) : "memory");
}
template <int OFF> DEV void as128(u4& d, u4 rs, u32 v, u32 s) { asm volatile("buffer_load_dwordx4 %0, %1, %2, %3 offen offset:%4 sc1" : "=v"(d) : "v"(v), "s"(rs), "s"(s), "i"(OFF) : "memory"); }
template <int OFF> DEV void as64(u2& d, u4 rs, u32 v, u32 s) { asm volatile("buffer_load_dwordx2 %0, %1, %2, %3 offen offset:%4 sc1" : "=v"(d) : "v"(v), "s"(rs), "s"(s), "i"(OFF) : "memory"); }
template <int OFF> DEV void al128(u4& d, u4 rs, u32 v, u32 s) { asm volatile("buffer_load_dwordx4 %0, %1, %2, %3 offen offset:%4" : "=v"(d) : "v"(v), "s"(rs), "s"(s), "i"(OFF) : "memory"); }
template <int OFF> DEV void al64(u2& d, u4 rs, u32 v, u32 s) { asm volatile("buffer_load_dwordx2 %0, %1, %2, %3 offen offset:%4" : "=v"(d) : "v"(v), "s"(rs), "s"(s), "i"(OFF) : "memory"); }
DEV void al16(u32& d, u4 rs, u32 v, u32 s) { asm volatile("buffer_load_ushort %0, %1, %2, %3 offen" : "=v"(d) : "v"(v), "s"(rs), "s"(s) : "memory"); }
template <int AUX> DEV u4 ld128(rsrc_t r, u32 v, u32 s) { return __builtin_amdgcn_raw_buffer_load_b128(r, v, s, AUX); }
template <int AUX> DEV u2 ld64(rsrc_t r, u32 v, u32 s) { return __builtin_amdgcn_raw_buffer_load_b64(r, v, s, AUX); }
template <int AUX> DEV void st128(u4 d, rsrc_t r, u32 v) { __builtin_amdgcn_raw_buffer_store_b128(d, r, v, 0, AUX); }
template <int AUX> DEV void st64(u2 d, rsrc_t r, u32 v, u32 s) { __builtin_amdgcn_raw_buffer_store_b64(d, r, v, s, AUX); }
DEV f16v mm32(u4 a, u4 b, f16v c) { return __builtin_amdgcn_mfma_f32_32x32x16_bf16(__builtin_bit_cast(bf8, a), __builtin_bit_cast(bf8, b), c, 0, 0, 0); }
DEV f4 mm16(u4 a, u4 b, f4 c) { return __builtin_amdgcn_mfma_f32_16x16x32_bf16(__builtin_bit_cast(bf8, a), __builtin_bit_cast(bf8, b), c, 0, 0, 0); }
DEV f4 mm4(u2 a, f4 c) { return __builtin_amdgcn_mfma_f32_4x4x4bf16_1k(__builtin_bit_cast(s4, a), __builtin_bit_cast(s4, a), c, 0, 0, 0); }
DEV void mv0(f16v& d, u4 a, u4 b) { asm volatile("v_mfma_f32_32x32x16_bf16 %0, %1, %2, 0" : "=&v"(d) : "v"(a), "v"(b)); }
DEV void mv(f16v& d, u4 a, u4 b) { asm volatile("v_mfma_f32_32x32x16_bf16 %0, %1, %2, %0" : "+v"(d) : "v"(a), "v"(b)); }
DEV void mv0a(f16v& d, u4 a, u4 b) { asm volatile("v_mfma_f32_32x32x16_bf16 %0, %1, %2, 0" : "=&v"(d) : "v"(a), "a"(b)); }
DEV void mva(f16v& d, u4 a, u4 b) { asm volatile("v_mfma_f32_32x32x16_bf16 %0, %1, %2, %0" : "+v"(d) : "v"(a), "a"(b)); }
DEV void mvca(f16v& d, u4 a, u4 b, const f16v& c) { asm volatile("v_mfma_f32_32x32x16_bf16 %0, %1, %2, %3" : "=&v"(d) : "v"(a), "a"(b), "v"(c)); }
DEV void mw0(f16v& d, u4 a, u4 b) { asm volatile("v_mfma_f32_32x32x16_bf16 %0, %1, %2, 0" : "=&v"(d) : "a"(a), "v"(b)); }
DEV void mw(f16v& d, u4 a, u4 b) { asm volatile("v_mfma_f32_32x32x16_bf16 %0, %1, %2, %0" : "+v"(d) : "a"(a), "v"(b)); }
DEV void mx0(f16v& d, u4 a, u4 b) { asm volatile("v_mfma_f32_32x32x16_bf16 %0, %1, %2, 0" : "=&a"(d) : "a"(a), "v"(b)); }
DEV void mx(f16v& d, u4 a, u4 b) { asm volatile("v_mfma_f32_32x32x16_bf16 %0, %1, %2, %0" : "+a"(d) : "a"(a), "v"(b)); }
DEV void mv16(f4& d, u4 a, u4 b) { asm volatile("v_mfma_f32_16x16x32_bf16 %0, %1, %2, %0" : "+v"(d) : "v"(a), "v"(b)); }
DEV void mv160(f4& d, u4 a, u4 b) { asm volatile("v_mfma_f32_16x16x32_bf16 %0, %1, %2, 0" : "=&v"(d) : "v"(a), "v"(b)); }
template <int N> DEV void nop() { asm volatile("s_nop %0" ::"i"(N)); }
DEV u32 pk(float lo, float hi) { return __builtin_bit_cast(u32, __builtin_convertvector((f2){lo, hi}, bf2)); }
DEV float asf(u32 d) { return __builtin_bit_cast(float, d); }
DEV u32 asu(float d) { return __builtin_bit_cast(u32, d); }
DEV float blo(u32 d) { return asf(d << 16); }
DEV float bhi(u32 d) { return asf(d & 0xffff0000u); }
DEV float rbf(float x) { return asf(pk(0.f, x)); }
DEV f2 up(u32 d) { return (f2){blo(d), bhi(d)}; }
DEV u4 q4(const f16v& a, int n) { return __builtin_bit_cast(u4, (f4){a[4 * n], a[4 * n + 1], a[4 * n + 2], a[4 * n + 3]}); }
DEV float xhalf(float v) {
  auto r = __builtin_amdgcn_permlane32_swap(asu(v), asu(v), false, false);
  return asf(r[0]) + asf(r[1]);
}
DEV bool anyl(bool c) { return __builtin_amdgcn_ballot_w64(c) != 0; }
DEV u32 mx3(u32 a, u32 b, u32 c) { return max(max(a, b), c); }
DEV u32 mx4(u32 m, u4 v) { return mx3(mx3(m, v[0], v[1]), v[2], v[3]); }
DEV u64 bal(bool c) { u64 m = __builtin_amdgcn_ballot_w64(c); asm volatile("" : "+s"(m)); return m; }
DEV u64 fbad(u2 f, u2 tag) { return bal(__builtin_bit_cast(u64, f) != __builtin_bit_cast(u64, tag)); }
DEV void wait_flags(u64 bad, u4 rs, u32 voff, u32 so, u2 tag) {
  if (__builtin_expect(bad != 0, 0)) {
    COLD();
#pragma nounroll
    for (u32 it = 0; it < SPIN; ++it) {
      u2 f;
      as64<0>(f, rs, voff, so);
      vmw<0>();
      KEEP(f);
      if (!fbad(f, tag)) break;
      __builtin_amdgcn_s_sleep(1);
    }
  }
  FENCE();
}
DEV u2 next_tag(u64 e) {
  e = e * 6364136223846793005ull + 1442695040888963407ull;
  return (u2){(u32)e, (u32)(e >> 32)};
}

#define STAMP(k)
#define STAMPD(k)

DEV f2 gelu2(f2 p, float ca, float cb, float inv) {
  f2 t = p * (p * p * cb + ca);
  f2 e = {__builtin_amdgcn_exp2f(t[0]), __builtin_amdgcn_exp2f(t[1])};
  f2 d = e * inv + inv;
  f2 q = {__builtin_amdgcn_rcpf(d[0]), __builtin_amdgcn_rcpf(d[1])};
  return p * q;
}

constexpr int cta[36] = {0, 0, 1, 1, 0, 2, 1, 2, 2, 0, 3, 1, 3, 2, 3, 3, 0, 4, 1, 4, 2, 4, 3, 4, 0, 5, 1, 5, 2, 5, 0, 6, 1, 6, 0, 7};
constexpr int ctb[36] = {0, 1, 0, 1, 2, 0, 2, 1, 2, 3, 0, 3, 1, 3, 2, 3, 4, 0, 4, 1, 4, 2, 4, 3, 5, 0, 5, 1, 5, 2, 6, 0, 6, 1, 7, 0};
constexpr bool cfirst(int k) { for (int i = 0; i < k; ++i) if (cta[i] + ctb[i] == cta[k] + ctb[k]) return false; return true; }
constexpr int clast(int n) { int r = 0; for (int i = 0; i < 36; ++i) if (cta[i] + ctb[i] == n) r = i; return r; }
template <int NR, class F> DEV void conv_run(f4 (&acc)[8], u32 ha, u32 ua, F&& side) {
  u4 A[NR][8], B[NR][8];
#pragma unroll
  for (int a = 0; a < 8; ++a) { A[0][a] = lr128a8(ha + 512 * a); B[0][a] = lr128(ua + 512 * (7 - a)); }
  SB();
  sfor<0, NR * 36>([&](auto ii) AI {
    constexpr int r = decltype(ii)::v / 36, k = decltype(ii)::v % 36;
    if constexpr (r + 1 < NR && k >= 4 && (k & 1) == 0) {
      constexpr int f = (k - 4) >> 1, a = f >> 1;
      if constexpr (f & 1) B[r + 1][a] = lr128(ua + 512 * (7 - a) + 64 * (r + 1));
      else A[r + 1][a] = lr128a8(ha + 512 * a + 64 * (r + 1));
    }
    if constexpr (k & 1) side(IC<r * 18 + (k >> 1)>{});
    acc[cta[k] + ctb[k]] = mm16(A[r][cta[k]], B[r][ctb[k]], acc[cta[k] + ctb[k]]);
    SB();
  });
}
DEV void put_copies(u32 base, u4 d, u2 p) {
  u32 e0 = __builtin_amdgcn_alignbit(p[1], p[0], 16), e1 = __builtin_amdgcn_alignbit(d[0], p[1], 16);
  u32 e2 = __builtin_amdgcn_alignbit(d[1], d[0], 16), e3 = __builtin_amdgcn_alignbit(d[2], d[1], 16);
  u32 e4 = __builtin_amdgcn_alignbit(d[3], d[2], 16);
  lw128(base, (u4){e0, e1, e2, e3});
  lw128(base + 4128, (u4){p[1], d[0], d[1], d[2]});
  lw128(base + 8256, (u4){e1, e2, e3, e4});
  lw128(base + 12384, d);
}
template <int N> DEV u4 sub4(const u16v& v) { return __builtin_shufflevector(v, v, 4 * N, 4 * N + 1, 4 * N + 2, 4 * N + 3); }

constexpr bool cfree(int s) { return (CM >> s & 1) && s != F1S && s != FCS && !(s >= SG && s < SG + 7); }
constexpr int cidx(int s) { int n = NK + NB; for (int i = 0; i < s; ++i) n += cfree(i); return n + (s >= DS ? ND : 0); }
constexpr int ridx(int h) { int n = 0; for (int i = 0; i < h; ++i) n += RM >> i & 1; return n; }
static_assert(PFS == 8 && DS >= 20 && DS + 3 < 26 && SG + 6 < DS && cidx(28) <= SE && NK / 2 + NB <= 20 && FCS + 2 < FCC && FCC + 2 < F1S && FCC < DS);
constexpr int zoff(int s) { return s < 8 ? s * 8192 : ((s - 8) >> 2) * 131072 + (s & 3) * 16384; }

enum { VZ = 1, VS, VO, VC, VE, VH, VF2, VFY, VX, VY };
enum { JY, JB, JI, JX, JH, JG, NJ };
struct Plan { short vm[256]; bool head[256]; short at[NJ][257]; };
constexpr int jlen(int j) { return j == JY ? 45 : j == JB ? 16 : j == JX ? 26 : j == JH ? 26 : j == JG ? 224 : 16; }
constexpr int jcost(int j, int k) {
  if (j == JY) return k == 44 ? 9 : 1;
  if (j == JX) return k < 4 ? 1 : ((k - 4) % 11 == 3 || (k - 4) % 11 == 7 || (k - 4) % 11 == 10) ? 9 : 1;
  if (j == JI) return 1;
  if (j == JH) return k % 13 == 12 ? 9 : 1;
  if (j == JG) return (k % 14 >> 1) == 3 || (k % 14 >> 1) == 5 ? 2 : 1;
  return 1;
}
constexpr int jfrom(int j, int k) { return j == JY ? (k < 8 ? 16 : 37) : j == JB ? 100 : j == JI ? 144 : j == JX ? (k < 4 ? 120 : k < 15 ? 129 + XG * ((k - 4) >> 2) : X1 + XG1 * ((k - 15) >> 2)) : j == JH ? 194 : G0; }
constexpr int aidx(int i) { const int a = i - A0 - (i > 215) - (i > 231); return i >= A0 && (i & 15) != 7 && a < 28 ? a : -1; }
constexpr int fixed(int i) {
  const int s = i >> 4, g = i & 15;
  int c = (i + RA < 256) + ((i & 3) == 3);
  if (g == 7 && s < 15) c += 9;
  if (s + 1 < NCHK) c += g == 1 ? 3 : g == 4 ? 3 : g == 5 ? 1 : g == 6 ? 2 : 0;
  if (RE + 4 * s < 64) c += g == 9 ? 3 : g == 10 ? 2 : 0;
  if (i < 16 && (i & 1)) c += 9;
  if (i >= 24 && i < 32) c += 1;
  if (i == F2C || i == F2C + 2 || (HCM && (i == F3C || i == F3C + 1)) || i == E1 + 1 || i == E3 + 1 || i == E1 + 2 || i == E3 + 2) c += 2;
  if (i == 105 || i == E1 || i == E3) c += 9;
  if (aidx(i) >= 0) c += 2;
  return c;
}
constexpr Plan make_plan() {
  Plan p{};
  p.vm[F2L] = VF2 << 8;
  for (int k = 0; k < 3; ++k) { p.vm[E0 + 2 * k] = VE << 8 | k; p.vm[E2 + 2 * k] = VE << 8 | (3 + k); }
  p.vm[FYL] = VFY << 8;
  if (HCM) p.vm[F3L] = VX << 8;
  for (int k = 0; k < HN; ++k) p.vm[H0AT + HG * k] = VH << 8 | k;
  int z = 4 * PZ, w = SE, o = 0, last = 0;
  for (int i = 0; i < 256; i += 2) {
    if (p.vm[i]) continue;
    int e = 0;
    if (z < 64 && ((z >> 2) < 5 || (i > 16 * ((z >> 2) - 4) + 7 && i > F2C + 2))) e = VZ << 8 | z++;
    else if (w < 64 && ((w & 3) || (i & 15) == 0) && ((w >> 2) < 12 || i >= 16 * ((w >> 2) - 3 - RE / 4) + 16)) e = VS << 8 | w++;
    else if (o < 28 - OP && ((o & 1) || (i & 3) == 0) && ((OM >> (i >> 2)) & 1) && i >= (o < 8 ? 56 : o < 20 ? 16 * (11 + (o >> 2) - RE / 4) + 20 : 16 * (7 + (o >> 2)) + 8)) e = VO << 8 | o++;
    p.vm[i] = e;
    const int k = e >> 8;
    if (k == VZ || k == VS || k == VO) { p.head[i] = last + 1 != e || (e & (k == VO ? 1 : 3)) == 0; last = e; }
  }
  if (z < 64 || w < 64 || o < 28 - OP) p.at[0][0] = -1;
  int cnt[256] = {}, cyc[256] = {};
  bool st[256] = {};
  for (int i = 0; i < 256; ++i) { cnt[i] = CAP - fixed(i) - (p.vm[i] ? 2 : 0) - (p.head[i] ? 2 : 0); cyc[i] = cnt[i] + !p.vm[i]; }
  for (int j = 0; j < NJ; ++j) {
    int k = j == JG ? 14 * GE : 0;
    for (int i = 0; i < 256; ++i) {
      if (j || i) p.at[j][i] = k;
      const int k0 = k;
      while (k < jlen(j) && i >= jfrom(j, k)) {
        const int c = jcost(j, k);
        if (c == 9) { if (k != k0 || p.vm[i] || st[i] || cnt[i] < 3) break; st[i] = true; cnt[i] -= 2; cyc[i] -= 3; }
        else { if (cnt[i] < 1 || cyc[i] < c) break; cnt[i] -= 1; cyc[i] -= c; }
        ++k;
      }
    }
    p.at[j][256] = k;
    if (k < jlen(j)) p.at[0][0] = -1;
  }
  return p;
}
constexpr Plan PL = make_plan();
static_assert(PL.at[0][0] == 0 && PL.at[JB][127] == 16 && PL.at[JG][193] >= 112 && PL.at[JI][E1 + 2] == 16 && E1 + 3 < 192 && E3 + 3 < 256 && A0 > 199 && A0 < 215);

DEV void code_fetch(u32 tid, u32 b, u64 pc0) {
  const u64 pc = pc0 & ~255ull;
  const u4 rC = {(u32)pc, (u32)(pc >> 32) & 0xffffu, CODE_BYTES, 0x00020000u};
  asm volatile("s_mov_b32 m0, 0x10000 ; @m0\n\ts_nop 0\n\tbuffer_load_dword %0, %1, %2 offen lds" ::"v"(tid < 16 ? tid * 128 : OOB), "s"(rC), "s"((b >> 3) * 2048) : "memory");
}
DEV void pf(u32& d, u4 rs, u32 voff, u32 so) { asm volatile("buffer_load_dword %0, %1, %2, %3 offen ; @PF" : "+v"(d) : "v"(voff), "s"(rs), "s"(so) : "memory"); }
#define BUF(n) const void* g_##n, u32 n##_n, u32 n##_f
__global__ __launch_bounds__(256, 1) void fused(BUF(x), BUF(wi), BUF(ws), BUF(wsp), BUF(k), BUF(v), BUF(sk), BUF(h), BUF(d), BUF(b), BUF(wo), BUF(o)) {
  const u32 b = blockIdx.x;
  const u64 pc0 = __builtin_amdgcn_s_getpc();
#define STAMPIN()
#define ENTRY() \
  u16v ka, kb, kc_; u64 st64a, st64b, st64c; \
  asm volatile("s_load_dwordx16 %0, %3, 0\n\ts_load_dwordx16 %1, %3, 64\n\ts_load_dwordx16 %2, %3, 128" \
               : "=&s"(ka), "=&s"(kb), "=&s"(kc_) : "s"(__builtin_amdgcn_kernarg_segment_ptr())); \
  STAMPIN(); \
  u32 tid = threadIdx.x; KEEP(tid); \
  code_fetch(tid, b, pc0); \
  const u32 l = tid & 63, w = __builtin_amdgcn_readfirstlane(tid >> 6); \
  const u32 i32 = l & 31, hh = l >> 5, i16 = l & 15, g4 = l >> 4;
#define STAMP0()
#define STAMPOUT()

  if (__builtin_expect(b < 224, 1)) {
    constexpr int SOFF = 0; (void)SOFF;
    ENTRY();
    const u32 xcd = b & 7, j = b >> 3;
    const u32 pl = (j * 37) >> 8, cl = j - pl * 7, p = 4 * (xcd >> 1) + pl, kap = xcd & 1, c = 2 * cl + kap;
    u32 lw4 = w * 4096, lw2 = w * 2048;
    const u32 pw = p * 4 + w;
    const u32 sxa = ((l & 7) ^ (l >> 4)) << 4, sxw = ((l & 7) ^ (((l >> 4) & 1) << 2)) << 4;
    u32 vx[4], vwi[2];
#pragma unroll
    for (int r = 0; r < 4; ++r) vx[r] = (l >> 3) * 896 + (sxa ^ ((r & 1) << 6)) + r * 6144;
    vwi[0] = (l >> 3) * 3584 + sxw; vwi[1] = vwi[0] + 27648;
    u32 sx[7], sw[7];
#pragma unroll
    for (int r = 0; r < 7; ++r) {
      u32 q = r;
      if (AROT == 1) { q = cl + 2 * pl + r; q -= q >= 14 ? 14 : q >= 7 ? 7 : 0; }
      if (AROT == 2 && r) { q = cl + 2 * pl + r - 1; q -= q >= 12 ? 12 : q >= 6 ? 6 : 0; q += 1; }
      sx[r] = pw * 32 * 896 + q * 128; sw[r] = w * 16 * 3584 + c * 128 + q * 229376;
    }
    const u32 ch = 224 + xcd * 28 + j;
    u32 chd = (ch >> 1) * 4;
    KEEP(vx[0], vx[1], vx[2], vx[3], vwi[0], vwi[1]);
    asm volatile("s_waitcnt lgkmcnt(0)" : "+s"(ka), "+s"(kb), "+s"(kc_), "+s"(sx[0]), "+s"(sw[0]), "+s"(sx[1]), "+s"(sw[1]), "+s"(sx[2]), "+s"(sw[2]), "+s"(sx[3]), "+s"(sw[3]), "+s"(sx[4]), "+s"(sw[4]), "+s"(sx[5]), "+s"(sw[5]), "+s"(sx[6]), "+s"(sw[6]), "+s"(lw4), "+s"(lw2), "+s"(chd));
    STAMP0();
    const u4 dX = sub4<0>(ka), dWi = sub4<1>(ka), dW = sub4<2>(ka), dS = sub4<3>(ka);
    constexpr int sz[16] = {7, 8, 9, 0, 1, 2, 7, 8, 9, 0, 1, 2, 0, 1, 2, 0};
    constexpr u32 VB = 2 * ZN, KB = VB + 9216, EXA = KB + 8192;
    const u32 R0 = 128 * p + 64 * kap;
    u32 so, braw, l16, zo, kvo, fa, fb, fc;
    u2 taps;
    rsrc_t rW;
    auto bg = [&](auto ii) AI {
      constexpr int v = decltype(ii)::v, i = v;
      if constexpr (v == 61) {
        dma1<3 * ZN + 4096, 0>(sub4<3>(kb), l16, opqs(ch * 4096 + w * 1024), opqs(w * 1024));
        mark<MW1>();
      } else if constexpr (v > 61) {
      } else if constexpr (i < 42) {
        constexpr int kc = i / 6, r = i % 6;
        if constexpr (r == 0) { if constexpr (kc >= 3) lneed<17 + kc>(); so = dma1<(7 + kc % 3) * ZN, 0>(dX, vx[0], sx[kc], lw4); }
        else if constexpr (r < 4) dman<r * 1024>(dX, vx[r], so);
        else if constexpr (r == 4) so = dma1<(6 - kc) * 8192, 1792>(dWi, vwi[0], sw[kc], lw2);
        else { dman<1024>(dWi, vwi[1], so); mark<MA + kc>(); }
      } else if constexpr (i < 56) {
        constexpr int kc = (i - 42) >> 1;
        if constexpr (i & 1) dman<1024>(dWi, vwi[1], so);
        else so = dma1<(7 + kc) * 8192, 0>(dWi, vwi[0], sw[kc], lw2);
      } else if constexpr (i == 56) so = dma1<KB, 0>(sub4<0>(kb), vx[0], (R0 + 16 * w) * 896 + cl * 128, lw2);
      else if constexpr (i == 57) dman<1024>(sub4<0>(kb), vx[1], so);
      else if constexpr (i == 58) so = dma1<VB, 0>(sub4<1>(kb), vx[0], so, lw2);
      else if constexpr (i == 59) dman<1024>(sub4<1>(kb), vx[1], so);
      else if constexpr (i == 60) {
        asm volatile("s_mov_b32 m0, %0 ; @m0" ::"i"(KB - 2048));
        al64<0>(taps, sub4<2>(kb), i16 * 8, cl * 512 + w * 128);
        dman<1024>(sub4<0>(kb), vx[1] | ((R0 - 1) & OOB), opqs(max(R0, 16u)) * 896 + (cl * 128 - 16 * 896));
        al16(braw, sub4<1>(kc_), i32 * 2, pw * 64);
      }
      SB();
    };
    sfor<0, 6>(bg);
    u64 e0; u32 draw;
    asm volatile("s_load_dwordx2 %0, %2, 0\n\ts_load_dword %1, %3, %4" : "=&s"(e0), "=&s"(draw)
                 : "s"(((u64)dW[1] << 32) | dW[0]), "s"(((u64)kc_[1] << 32) | kc_[0]), "s"(chd));
    const u32 rdA = lw4 + i32 * 128 + ((hh ^ ((i32 >> 1) & 7)) << 4);
    u32 ra[4];
#pragma unroll
    for (int q = 0; q < 4; ++q) ra[q] = 7 * ZN + (rdA ^ ((q ^ w) << 5));
    KEEP(ra[0], ra[1], ra[2], ra[3]);
    SB();
    sfor<6, 8>(bg);
    const u32 trA = (8 * hh + ((l >> 2) & 3)) * 128 + (((g4 & 1) * 32 + (l & 3) * 8) ^ (((l >> 3) & 1) << 6));
    u32 ta[4][2];
#pragma unroll
    for (int q = 0; q < 4; ++q) { ta[q][0] = trA + ((q ^ w) << 11); ta[q][1] = ta[q][0] ^ 64; KEEP(ta[q][0], ta[q][1]); }
    SB();
    sfor<8, 10>(bg);
    const u32 selv = ((g4 ^ i16) & 1) ? 0u : 0x3f803f80u;
    u4 sel = {selv, selv, selv, selv};
    l16 = l * 16; zo = l16 + (WS_Z + c * 262144 + pw * 2048);
    KEEP(sel, l16, zo);
    SB();
    sfor<10, 12>(bg);
    rW = mk(dW);
    u32 vs[4];
    const u32 st0 = opqs(pw * 131072 + kap * 2048);
    kvo = i16 * 4096 - 16 * g4 + (WS_KV + (64 * cl + 16 * w) * 4096 + 4016 - 2 * R0);
    KEEP(kvo);
    SB();
    sfor<12, 18>(bg);
    STAMP(16);
    auto dmaS = [&](auto dd) AI {
      constexpr int m = (decltype(dd)::v & 255) >> 2, r = decltype(dd)::v & 3;
      if constexpr (r == 0 && m >= 6) lneed<m < 12 ? m - 6 : m - 3>();
      if constexpr (r == 0 || decltype(dd)::v >= 256) so = dma1<sz[m] * ZN, m * 128, r * 1024>(dS, vs[r], st0, lw4);
      else dman<r * 1024>(dS, vs[r], so);
      if constexpr (r == 3) mark<MS + m>();
      SB();
    };
    u4 xf[28], wf[28][2];
    f16v z2[2], z1[2];
    f4 sq[2] = {(f4)0.f, (f4)0.f}, sxs = (f4)0.f, cs[2] = {(f4)0.f, (f4)0.f};
    float mean, rstd, sdev, ca, cb;
    u4 mop, cop[2];
    auto cs_put = [&](u32 ex) AI {
      const float u0 = opf(cs[0][0]), u1 = opf(cs[0][1]), u2_ = opf(cs[1][0]), u3 = opf(cs[1][1]);
      const float c0 = (g4 & 2) ? u2_ : u0, c1 = (g4 & 2) ? u3 : u1;
      const float cv = (g4 & 1) ? c1 : c0;
      lw32(ex + l16 + w * 4, pk(cv, cv - rbf(cv)));
    };
    auto cs_get = [&](u32 ex) AI { cop[0] = lr128(ex + i32 * 16); cop[1] = lr128(ex + 512 + i32 * 16); };
    auto rdw = [&](int s, int n, int up = 0) AI { const u32 a = ta[s & 3][n] + (up ? s >> 2 : 6 - (s >> 2)) * 8192; wf[s][n] = ltr2(a, a + 512); };
    auto stat = [&](int f) AI {
      sxs = mm16(sel, xf[f], sxs);
      sq[0] = mm4((u2){xf[f][0], xf[f][1]}, sq[0]);
      sq[1] = mm4((u2){xf[f][2], xf[f][3]}, sq[1]);
      SB();
    };
    u2 kq[2][3], vq[2][2];
    vw<MA>();
#pragma unroll
    for (int q = 0; q < 4; ++q) xf[q] = lr128(ra[q]);
    lmark<20>();
    barv();
    STAMP(17);
    rdw(0, 0); rdw(0, 1); rdw(1, 0); rdw(1, 1);
    SB();
    u2 tag;
    float dv, t0, t1;
    sfor<0, 28>([&](auto ss) AI {
      constexpr int s = decltype(ss)::v, kc = s >> 2, q = s & 3;
      constexpr int o = kc < 4 ? 18 + 6 * kc + (q < 2 ? 2 * q : 2 + q) : 42 + 2 * (s - 16);
      if constexpr (s == 0) mv0(z2[0], wf[s][0], xf[s]); else mv(z2[0], wf[s][0], xf[s]);
      if constexpr (s == 27) mv(z2[0], cop[0], mop);
      if constexpr (s + 2 < 28) rdw(s + 2, 0);
      SB();
      bg(IC<o>{});
      if constexpr (s == 26) {
        const float u0 = opf(sxs[0]), u1 = opf(sxs[1]);
        const float m = ((g4 & 1) ? u1 : u0) * (1.f / 448.f);
        t0 = fmaxf(opf(xhalf(t0) * (1.f / 448.f)) - m * m, 0.f);
        const u32 mh = pk(-m, -m);
        mean = -bhi(mh);
        const u32 mz = hh ? 0u : mh;
        mop = (u4){mz, mz, mz, mz};
        KEEP(t0); KEEP(mop);
      }
      if constexpr (s == 27) {
        sdev = rbf(__builtin_amdgcn_sqrtf(rbf(rbf(t0) + 1e-5f)));
        rstd = __builtin_amdgcn_rcpf(sdev);
        KEEP(rstd);
      }
      SB();
      if constexpr (q == 0) { cs[0] = mm16(sel, wf[s][0], cs[0]); SB(); }
      if constexpr (s == 0) mv0(z2[1], wf[s][1], xf[s]); else mv(z2[1], wf[s][1], xf[s]);
      if constexpr (s == 27) mv(z2[1], cop[1], mop);
      if constexpr (s + 2 < 28) rdw(s + 2, 1);
      SB();
      if constexpr (q < 2 || kc >= 4) bg(IC<o + 1>{});
      if constexpr (q == 1 && kc < 6) {
        vw<MA + kc + 1>();
#pragma unroll
        for (int r = 0; r < 4; ++r) xf[4 * kc + 4 + r] = lr128(ra[r] + ((kc + 1) % 3) * ZN);
        if constexpr (kc < 3) lmark<21 + kc>();
        barv();
        SB();
      }
      if constexpr (q == 0) { cs[1] = mm16(sel, wf[s][1], cs[1]); SB(); }
      if constexpr (s == 2) { stat(0); stat(1); }
      if constexpr (q < 2 && s < 24) stat(s + 2);
      if constexpr (q == 3 && s < 24) { stat(s + 1); stat(s + 2); }
      if constexpr (s == 23) stat(26);
      if constexpr (s == 24) stat(27);
      if constexpr (s == 25) {
        float u[4];
#pragma unroll
        for (int r = 0; r < 4; ++r) u[r] = opf(opf(sq[0][r]) + opf(sq[1][r]));
        const float a0 = (l & 1) ? u[1] : u[0], a1 = (l & 1) ? u[3] : u[2];
        t0 = (l & 2) ? a1 : a0;
        KEEP(t0);
        cs_put(EXA);
      }
      if constexpr (s == 26) {
        bar(); ties(e0, draw);
        cs_get(EXA);
        tag = next_tag(e0);
        dv = asf((draw >> ((ch & 1) * 16)) << 16);
      }
      if constexpr (s == 27) {
        ca = -2.302208198f * rstd; cb = -0.1029432397f * rstd * rstd * rstd;
        KEEP(ca); KEEP(cb);
#pragma unroll
        for (int r = 0; r < 4; ++r) vs[r] = opq((l >> 3) * 4096 + (sxa ^ ((r & 1) << 6)) + r * 31744);
        KEEP(vs[0], vs[1], vs[2], vs[3]);
      }
      SB();
      if constexpr (q == 3) STAMP(48 + kc);
    });
    const u32 ho = opq(l16 + (WS_H + pw * 57344 + c * 2048));
    {
      f2 a1 = {0.f, 0.f}, a2 = {0.f, 0.f};
      sfor<0, 8>([&](auto ii) AI {
        constexpr int i = decltype(ii)::v, n = i >> 2, r = (i & 3) * 4;
        f2 u = gelu2((f2){z2[n][r], z2[n][r + 1]}, ca, cb, sdev), v = gelu2((f2){z2[n][r + 2], z2[n][r + 3]}, ca, cb, sdev);
        z2[n][r] = u[0]; z2[n][r + 1] = u[1]; z2[n][r + 2] = v[0]; z2[n][r + 3] = v[1];
        SB();
        if constexpr (NB == 16) dmaS(IC<2 * i>{}); else if constexpr (NB == 8) dmaS(IC<i>{});
        a1 += u + v; a2 += u * u;
        SB();
        a2 += v * v;
        SB();
        if constexpr (NB == 16) dmaS(IC<2 * i + 1>{});
        if constexpr (i == BW) {
          vw<MW1>();
          KEEP(taps, braw);
          barv();
        }
        if constexpr (i == 7) {
          const u32 kta = opq(VB + (8 * g4 + ((l >> 2) & 3)) * 128 + (((2 * w + ((l >> 1) & 1)) ^ ((l >> 3) & 1) ^ ((g4 & 1) << 2)) << 4) + (l & 1) * 8);
#pragma unroll
          for (int h = 0; h < 2; ++h) {
            kq[h][0] = ltr((kta ^ 96) + (KB - VB + 4096 * h - 512)); kq[h][1] = ltr(kta + (KB - VB + 4096 * h)); kq[h][2] = ltr((kta ^ 32) + (KB - VB + 4096 * h + 512));
            vq[h][0] = ltr(kta + 4096 * h); vq[h][1] = ltr((kta ^ 32) + (4096 * h + 512));
          }
        }
        SB();
        if constexpr (i == 3) STAMP(55);
      });
      float s1 = xhalf(a1[0] + a1[1]), s2 = xhalf(a2[0] + a2[1]);
      st128<SC1>((u4){asu(s1), tag[0], asu(s2), tag[1]}, rW, (hh ? OOB : l16) + (WS_ST + (pw * 14 + c) * 512));
      SB();
    }
    STAMP(2);
    {
      const float tp[4] = {blo(taps[0]), bhi(taps[0]), blo(taps[1]), bhi(taps[1])};
      sfor<0, 2>([&](auto hh_) AI {
        constexpr int h = decltype(hh_)::v;
        float kf[11], o[8];
        if constexpr (NK > 0) dmaS(IC<NB + NK / 2 * h>{});
        u4 sent = {SENT, SENT, SENT, SENT};
#pragma unroll
        for (int e = 0; e < 11; ++e) { const int x = e + 1; const u32 d = kq[h][x >> 2][(x >> 1) & 1]; kf[e] = (x & 1) ? bhi(d) : blo(d); }
        sfor<0, 8>([&](auto nn) AI {
          constexpr int n = decltype(nn)::v;
          const u32 d = vq[h][n >> 2][(n >> 1) & 1];
          o[n] = opf(__builtin_fmaf(kf[n + 3], tp[0], __builtin_fmaf(kf[n + 2], tp[1], __builtin_fmaf(kf[n + 1], tp[2], kf[n] * tp[3]))) * ((n & 1) ? bhi(d) : blo(d)));
          if constexpr ((n & 1) && n / 2 + 1 < NK / 2) { KEEP(o[n - 1], o[n]); SB(); dmaS(IC<NB + NK / 2 * h + n / 2 + 1>{}); }
          if constexpr (HCM == 0 && (n == 3 || n == 5)) { KEEP(o[n - 1], o[n]); SB(); st128<SC1>(sent, rW, ho + h * 28672 + (n - 3) * 512); SB(); }
        });
        st128<SC1>((u4){pk(o[7], o[6]), pk(o[5], o[4]), pk(o[3], o[2]), pk(o[1], o[0])}, rW, kvo + 64 - 64 * h);
        SB();
        if constexpr (h == 0) {
          KEEP(kq[1][0], kq[1][1], kq[1][2], vq[1][0], vq[1][1]);
          barv();
#pragma unroll
          for (int q = 0; q < 4; ++q) { ta[q][0] = opq(ta[q][0] + 57344); ta[q][1] = opq(ta[q][1] + 57344); }
          rdw(0, 0, 1); rdw(0, 1, 1); rdw(1, 0, 1); rdw(1, 1, 1);
          SB();
        }
      });
      mark<MZC>();
    }
    STAMP(56);
#define LANE() u32 l = l16; KEEP(l); l >>= 4; const u32 i32 = l & 31, hh = l >> 5, i16 = l & 15, g4 = l >> 4; (void)i32; (void)hh; (void)i16; (void)g4
    u4 Wf[64];
    u32 rs[2][4];
    auto rdS = [&](auto mm) AI {
      constexpr int e = decltype(mm)::v, m = e >> 2;
      if constexpr ((e & 3) == 0) vw<MS + m>();
      Wf[e] = lr128(rs[sz[m] >= 7][e & 3] + (sz[m] % 7) * ZN);
      if constexpr ((e & 3) == 3) lmark<m>();
      SB();
    };
    u4 gq[7], hd, ud;
    u2 hp, f1, ms;
    u32 go, f1o, uo, hla;
    u64 bad;
    const u32 gs = WS_ST + pw * 7168;
    cs[0] = (f4)0.f; cs[1] = (f4)0.f;
    constexpr u32 cb0 = 3 * ZN + 4096, ub = cb0 + 16512;
    constexpr int SD = cidx(DS) - ND, SC2 = cidx(28), RC = ridx(56);
    auto phaseD = [&]() AI {
      STAMP(1);
      auto gbad = [&]() AI -> bool {
        u32 bad = 0;
#pragma unroll
        for (int n = 0; n < 7; ++n) bad |= (gq[n][1] ^ tag[0]) | (gq[n][3] ^ tag[1]);
        return anyl(opq(bad) != 0);
      };
      auto gtie = [&]() AI { KEEP(gq[0], gq[1], gq[2], gq[3], gq[4], gq[5], gq[6]); };
      LANE();
      const u32 tb = 5 * ZN + lw4;
      const u32 sg = (i32 & 5) | ((i32 & 2) << 2) | ((i32 & 8) >> 2);
      u32 twr = tb + i32 * 128 + ((hh ^ sg) << 3);
      const u32 qq = (l >> 2) & 3;
      u32 trd = tb + (8 * hh + qq) * 128 + (((4 * (g4 & 1) + (l & 3)) ^ ((qq & 1) | ((qq & 2) << 2) | (hh << 1))) << 3);
      KEEP(twr, trd);
      sfor<SD, SD + ND>(dmaS);
      vw<MG>();
      gtie();
      if (__builtin_expect(gbad(), 0)) {
        COLD();
#pragma nounroll
        for (u32 it = 0; it < SPIN; ++it) {
          sfor<0, 7>([&](auto nn) AI { as128<decltype(nn)::v * 512>(gq[decltype(nn)::v], dW, go, gs); });
          vmw<0>();
          gtie();
          if (!gbad()) break;
        }
      }
      STAMP(5);
      float s1 = 0.f, s2 = 0.f;
#pragma unroll
      for (int n = 0; n < 7; ++n) { s1 += asf(gq[n][0]); s2 += asf(gq[n][2]); }
      s1 = xhalf(s1) * (1.f / 896.f); s2 = xhalf(s2) * (1.f / 896.f);
      const float r2 = __builtin_amdgcn_rcpf(rbf(__builtin_amdgcn_sqrtf(rbf(rbf(fmaxf(s2 - s1 * s1, 0.f)) + 1e-5f)))), o2 = -rbf(s1) * r2;
      u4 zt[2][2];
#pragma unroll
      for (int n = 0; n < 2; ++n) {
#pragma unroll
        for (int rq = 0; rq < 4; ++rq) {
          f2 a = {z2[n][4 * rq], z2[n][4 * rq + 1]}, bq = {z2[n][4 * rq + 2], z2[n][4 * rq + 3]};
          a = a * r2 + o2; bq = bq * r2 + o2;
          lw64(twr ^ ((8 * n + 2 * rq) << 3), (u2){pk(a[0], a[1]), pk(bq[0], bq[1])});
        }
        FENCE();
#pragma unroll
        for (int q = 0; q < 2; ++q) { const u32 a = (trd ^ (n << 6)) + q * 2048; zt[n][q] = ltr2(a, (a ^ 32) + 512); }
        FENCE();
        SB();
      }
#pragma unroll
      for (int n = 0; n < 2; ++n)
#pragma unroll
        for (int q = 0; q < 2; ++q) st128<SC1>(zt[n][q], rW, zo + n * 131072 + q * 1024);
      mark<MZS>();
      SB();
      STAMP(6);
    };
    sfor<0, 28>([&](auto ss) AI {
      constexpr int s = decltype(ss)::v;
      if constexpr (s == DS) phaseD();
      if constexpr (s == 0) mv0(z1[0], wf[s][0], xf[s]); else mv(z1[0], wf[s][0], xf[s]);
      if constexpr (s + 2 < 28) rdw(s + 2, 0, 1);
      SB();
      if constexpr (s == 0) {
#pragma unroll
        for (int q = 0; q < 4; ++q) { rs[0][q] = opq(rdA ^ (q << 5)); rs[1][q] = opq(rs[0][q] + 7 * ZN); }
      }
      if constexpr (cfree(s)) dmaS(IC<cidx(s)>{});
      if constexpr (s == F1S) { as64<0>(f1, dW, f1o, 0); mark<MF1>(); }
      if constexpr (s == HS) { hla = opq(l16 + (cb0 + w * 1024)); hd = lr128(hla); hp = lr64(hla - 8); lmark<32>(); }
      if constexpr (s == FCS) { as64<0>(f1, dW, fb, 0); mark<MF3>(); }
      if constexpr (s >= SG && s < SG + 7) { as128<(s - SG) * 512>(gq[s - SG], dW, go, gs); if constexpr (s == SG + 6) mark<MG>(); }
      if constexpr (RM >> (2 * s) & 1) rdS(IC<ridx(2 * s)>{});
      if constexpr (s == SG - 1) go = opq(hh * 3584 + i32 * 16);
      if constexpr (s == FCC) { vw<MF3>(); KEEP(f1); bad = fbad(f1, tag); }
      if constexpr (s == 25) cs_put(EXA + 1024);
      SB();
      if constexpr ((s & 3) == 0) { cs[0] = mm16(sel, wf[s][0], cs[0]); SB(); }
      if constexpr (s == 0) mv0(z1[1], wf[s][1], xf[s]); else mv(z1[1], wf[s][1], xf[s]);
      if constexpr (s + 2 < 28) rdw(s + 2, 1, 1);
      SB();
      if constexpr (RM >> (2 * s + 1) & 1) rdS(IC<ridx(2 * s + 1)>{});
      if constexpr (s == FS) { vw<MZC>(); st64<SC1>(tag, rW, opq(l16 >= 16 * FL ? OOB : l16 >> 1), WS_F1 + b * 512 + w * 128); }
      if constexpr (s == FCS - 1) fb = opq(i16 * 128 + opqs(WS_FZ + cl * 2048));
      if constexpr (s == FCC) wait_flags(bad, dW, fb, 0, tag);
      if constexpr (s == F1S - 2) fa = opq((l & 7) * 512 + opqs(WS_F1 + (ch >> 6) * 4096 + ((ch >> 4) & 3) * 128));
      if constexpr (s == F1S - 1) { fc = opq(((l >> 3) & 3) * 28672); f1o = opq(fa + fc); }
      if constexpr (s == 27) uo = opq((4080 - w * 1024) - l16);
      if constexpr (s == DS - 1) { lneed<32>(); barv(); }
      if constexpr (s == DS + 1) { const u32 hm = hla == cb0 ? 0 : ~0u; put_copies(hla + 32, hd, (u2){hp[0] & hm, hp[1] & hm}); }
      if constexpr (s == DS + 2) lw128(opq((cb0 + (l >> 1) * 4128 + (l & 1) * 16) & -(u32)(l < 8)) + opq((ub + 4096 + i32 * 16) & -(u32)(l >= 8)), (u4){0, 0, 0, 0});
      if constexpr (s == 24) lw64(EXA + 2048 + w * 256 + i32 * 8, (u2){asu(mean), asu(rstd)});
      if constexpr (s == 26) { bar(); cs_get(EXA + 1024); ms = lr64(EXA + 2048 + (2 * kap + (w >> 1)) * 256 + i32 * 8); }
      SB();
      if constexpr ((s & 3) == 0) { cs[1] = mm16(sel, wf[s][1], cs[1]); SB(); }
      if constexpr ((s & 7) == 7) STAMP(58 + (s >> 3));
    });
    mv(z1[0], cop[0], mop);
    mv(z1[1], cop[1], mop);
    STAMP(3);
    float bt;
    u32 zb[2], zc[2], zr[2], zm;
    u4 Zf[256];
    u32 zq[4];
    auto dmaZ = [&](auto dd) AI {
      constexpr int s = (decltype(dd)::v & 255) >> 2, r = decltype(dd)::v & 3;
      if constexpr (r == 0 && s >= 2 && s < 5) lneed<4 + s>();
      if constexpr (r == 0 || decltype(dd)::v >= 256) so = dmas<(5 + s % 5) * ZN, zoff(s), r * 1024>(dW, l16, zb[s >> 3], lw4);
      else dmasn<r * 1024>(dW, l16, so);
      if constexpr (r == 3) mark<MZ + s>();
      SB();
    };
    auto zchk = [&](auto ss, auto dd) AI {
      constexpr int s = decltype(ss)::v, d = decltype(dd)::v;
      auto rd = [&]() AI {
#pragma unroll
        for (int n = 0; n < 4; ++n) zq[n] = lr32(zc[(s % 5) >> 2] + ((s % 5) & 3) * ZN + n * 1024);
      };
      if constexpr (d == 0) { vw<MZ + s>(); rd(); FENCE(); }
      else if constexpr (d == 1) { zm = max(mx3(zq[0], zq[1], zq[2]), zq[3]); KEEP(zm); }
      else if constexpr (d == 2) bad = bal(zm == SENT);
      else if (__builtin_expect(bad != 0, 0)) {
        COLD();
        u32 m0s;
        asm volatile("s_mov_b32 %0, m0 ; @m0" : "=s"(m0s));
#pragma nounroll
        for (u32 it = 0; it < SPIN; ++it) {
          const u32 t = dmas<(5 + s % 5) * ZN, zoff(s)>(dW, l16, zb[s >> 3], lw4);
          dmasn<1024>(dW, l16, t); dmasn<2048>(dW, l16, t); dmasn<3072>(dW, l16, t);
          vmw<0>();
          rd();
          FENCE();
          if (!anyl(max(mx3(zq[0], zq[1], zq[2]), zq[3]) == SENT)) break;
        }
        asm volatile("s_mov_b32 m0, %0 ; @m0" ::"s"(m0s));
      }
    };
    auto rdZ = [&](auto ii) AI {
      constexpr int i = decltype(ii)::v, s = i >> 4, g = i & 15, z = s % 5;
      constexpr int o = s < 8 ? (2 * (g & 1) + (g >> 3)) * 4096 + ((g >> 1) & 3) * 1024 : g * 1024;
      Zf[i] = lr128(zr[z >> 2] + (z & 3) * ZN + o);
    };
    f4 cacc[8];
    u2 xv[4];
    u32 hb, xa, xs, tro[4], vo[2], ob[2], xo, fyo, f2o, f3o, eb, cw, ry[2], ya, ys, yo;
    float zf[32];
    {
      vw<MF1>();
      KEEP(f1);
      wait_flags(fbad(f1, tag), dW, f1o, 0, tag);
      as128<0>(ud, dW, uo, WS_KV + ch * 4096);
      mark<MKV>();
      vneed<MZS>();
      st64<SC1>(tag, rW, opq(l16 >= 16 * FL ? OOB : l16 >> 1), WS_F2 + b * 512 + w * 128);
      STAMP(4);
      LANE();
      auto eS = [&](auto ii) AI { if constexpr (SC2 + decltype(ii)::v < SE) dmaS(IC<SC2 + decltype(ii)::v>{}); };
      sfor<0, 2>(eS);
      sfor<RC, (RC + RE) / 2>(rdS);
      bt = blo(braw);
      zb[0] = opqs(WS_Z + (c ^ 1) * 262144 + (w >> 1) * 131072 + kap * 65536 + (w & 1) * 4096);
      zb[1] = opqs(WS_Z + c * 262144 + kap * 65536 + w * 4096);
      zr[0] = l16 + 5 * ZN; zr[1] = l16 + 9 * ZN; zc[0] = zr[0] + lw4 + ((l >> 1) & 12); zc[1] = zc[0] + 4 * ZN;
      KEEP(bt, zr[0], zr[1], zc[0], zc[1]);
      SB();
      const u32 T0 = 128 * p + 64 * kap + 32 * (w >> 1), D0 = 64 * cl + 32 * (w & 1);
      xa = i32 * 896 + hh * 8; xs = opqs(T0 * 896 + D0 * 2);
      cw = 3 * ZN + lw4 + i16 * 64 + ((g4 ^ ((i16 >> 1) & 3)) << 4);
      ry[0] = (3 * ZN + (w >> 1) * ZN + (w & 1) * 2048) + ((2 * l16) ^ ((l16 >> 2) & 48)); ry[1] = ry[0] ^ 16;
      KEEP(xa, cw, ry[0], ry[1]);
      SB();
      eS(IC<2>{});
      vo[0] = (l >> 3) * 896 + (((l & 7) ^ ((g4 & 1) << 2)) << 4); vo[1] = vo[0] + 8 * 896 - 1024;
      yo = l16 + (WS_Y + ch * 4096 + w * 1024); xo = l16 + (WS_X + (b * 4 + w) * 2 * XS);
      ob[0] = opqs((448 * (w & 1) + 208 * (w >> 1)) * 896 + cl * 128); ob[1] = opqs((448 * (w & 1) + 240 * (w >> 1) - 16) * 896 + cl * 128);
      KEEP(vo[0], vo[1], yo, xo);
      SB();
      eS(IC<3>{});
      f2o = fc + (l & 6) * 256 + (l & 1) * 128 + (WS_F2 + cl * 4096 + kap * 2048 + (w & 1) * 256);
      if constexpr (HCM == 0) {
        fa = opq(min(i32 >> 2, 6u) * 4096 + (l & 2) * 256 + (l & 1) * 128 + (WS_F2 + pl * 28672 + (xcd & 6) * 512 + kap * 256));
        f2o = opq(f2o); f2o = hh ? f2o : fa;
      } else { f3o = (l & 7) * 128 + (WS_FH + p * 1024); KEEP(f3o); }
      fyo = WS_FY + (D0 + i32) * 128;
      fa = (l16 | (w << 10)) >= 16 * FL ? OOB : l16 >> 1;
      eb = opqs(WS_X + ((b ^ 1) * 4 + w) * 2 * XS); ys = opqs(WS_Y + D0 * 4096 + T0 * 2);
      KEEP(fyo, f2o, fa);
      SB();
      eS(IC<4>{});
      tro[0] = (lw4 & 4096) + (4 * hh + ((l >> 2) & 3)) * 128 + (((g4 & 1) * 32 + (l & 3) * 8) ^ ((((l >> 3) ^ w) & 1) << 6));
      tro[1] = tro[0] ^ 64; tro[2] = tro[0] + 4 * ZN; tro[3] = tro[1] + 4 * ZN;
      ya = (l >> 2) * 4096 + (l & 3) * 16;
      hb = opqs(WS_H + (p * 4 + 2 * kap + (w >> 1)) * 57344 + (w & 1) * 14336);
      KEEP(tro[0], tro[1], tro[2], tro[3], ya);
      SB();
      eS(IC<5>{});
#pragma unroll
      for (int r = 0; r < 32; ++r) zf[r] = z1[r >> 4][r & 15];
#pragma unroll
      for (int r = 0; r < GE; ++r) { f2 u = gelu2((f2){zf[2 * r], zf[2 * r + 1]}, ca, cb, sdev); zf[2 * r] = u[0]; zf[2 * r + 1] = u[1]; KEEP(zf[2 * r], zf[2 * r + 1]); }
      SB();
      sfor<(RC + RE) / 2, RE>(rdS);
      sfor<6, 24>(eS);
      sfor<0, 4>([&](auto rr) AI { al64<decltype(rr)::v * 16>(xv[decltype(rr)::v], dX, xa, xs); });
      const u32 ha = opq(cb0 + (i16 & 3) * 4128 + 8 + 16 * g4 + 8 * (i16 >> 2) + 128 * w), ua = opq(ub + 480 - 32 * i16 + 16 * g4 + 128 * w);
      vw<MKV>();
      KEEP(ud);
      lw128(ub + uo, ud);
      bar();
      STAMP(7);
#pragma unroll
      for (int n = 0; n < 8; ++n) cacc[n] = (f4)0.f;
      conv_run<2>(cacc, ha, ua, [&](auto kk) AI {
        constexpr int k = decltype(kk)::v;
        if constexpr (k >= ZK && k < ZK + 8 * PZ && ((k - ZK) & 1) == 0) dmaZ(IC<(k - ZK) / 2>{});
        if constexpr (k == 25) zchk(IC<0>{}, IC<0>{});
        if constexpr (k == 28) zchk(IC<0>{}, IC<1>{});
        if constexpr (k == 29) zchk(IC<0>{}, IC<2>{});
        if constexpr (k == 30) zchk(IC<0>{}, IC<3>{});
        if constexpr (k == 31) barv();
        if constexpr (k > 31) sfor<2 * (k - 32), 2 * (k - 32) + 2>(rdZ);
      });
    }
    STAMP(9);
    u4 Hf[28], yr[2], Af[56];
    u2 fy;
    auto ldH = [&](auto ii) AI {
      constexpr int n = decltype(ii)::v / 14, m = decltype(ii)::v % 14;
      as128<(m & 3) * 1024>(Hf[14 * n + m], dW, l16, hb + n * 28672 + (m & 12) * 1024);
      if constexpr (m & 1) mark<MH0 + 7 * n + m / 2>();
      SB();
    };
    constexpr int oz[7] = {3, 4, 1, 2, 0, 6, 7};
    auto dmaO = [&](auto dd) AI {
      constexpr int u = (decltype(dd)::v & 255) >> 1, r = decltype(dd)::v & 1, n = u / 7, m = u % 7;
      if constexpr (r == 0 && (u == 4 || u == 6 || u == 8)) lneed<11 + u / 2>();
      if constexpr (r == 0 || decltype(dd)::v >= 256) so = dma1<oz[u >> 1] * ZN + (u & 1) * 2048, (m + n) * 32 * 896, r * 1024>(sub4<2>(kc_), vo[r], ob[m & 1], lw4);
      else dman<1024>(sub4<2>(kc_), vo[1], so);
      if constexpr (r == 1) mark<MO + u>();
      SB();
    };
    auto rdO = [&](auto ii) AI {
      constexpr int f = decltype(ii)::v, m = f % 28 >> 1, u = f / 28 * 7 + m % 7, e = 1 - (f & 1), o = (oz[u >> 1] & 3) * ZN + (u & 1) * 2048 + m / 7 * 8192;
      const u32 a = tro[e + 2 * (oz[u >> 1] >> 2)] + o;
      Af[f] = ltr2(a, a + 1024);
    };
    {
      f4 yp[2][4];
      f16v pa[2], own[2];
      u4 pp[3], eo[3], hq, idm[2];
      float hv[8], yv[8], yu[8], bv[16], gt[2];
      u32 it0, it1;
      u2 f2v;
      u64 bad2;
      auto ldE = [&](auto ii) AI { constexpr int n = decltype(ii)::v / 3, r = decltype(ii)::v % 3; as128<r * 1024>(pp[r], dW, l16, eb + n * XS); };
      auto ebad = [&]() AI -> u64 { return bal(opq((pp[0][3] ^ tag[0]) | (pp[1][3] ^ tag[0]) | (pp[2][3] ^ tag[0]) | (pp[2][2] ^ tag[1])) != 0); };
      auto efix = [&](auto nn) AI {
        constexpr int n = decltype(nn)::v;
        if (__builtin_expect(bad != 0, 0)) {
          COLD();
#pragma nounroll
          for (u32 it = 0; it < SPIN; ++it) {
            sfor<3 * n, 3 * n + 3>(ldE);
            vmw<0>();
            KEEP(pp[0], pp[1], pp[2]);
            if (!ebad()) break;
          }
        }
      };
      auto job = [&](auto jj, auto kk) AI {
        constexpr int j = decltype(jj)::v, k = decltype(kk)::v;
        if constexpr (j == JG) {
          constexpr int r = k / 14 * 2 + (k & 1), o = k % 14 >> 1, e = k & 1;
          if constexpr (o == 0) gt[e] = zf[r] * zf[r];
          else if constexpr (o == 1) gt[e] = __builtin_fmaf(gt[e], cb, ca);
          else if constexpr (o == 2) gt[e] *= zf[r];
          else if constexpr (o == 3) gt[e] = __builtin_amdgcn_exp2f(gt[e]);
          else if constexpr (o == 4) gt[e] = __builtin_fmaf(gt[e], sdev, sdev);
          else if constexpr (o == 5) gt[e] = __builtin_amdgcn_rcpf(gt[e]);
          else zf[r] *= gt[e];
          if constexpr (o < 6) KEEP(gt[e]); else KEEP(zf[r]);
        } else if constexpr (j == JY) {
          constexpr int r = k & 7;
          if constexpr (k < 8) { yu[r] = (r & 1) ? blo(ud[3 - (r >> 1)]) : bhi(ud[3 - (r >> 1)]); KEEP(yu[r]); }
          else if constexpr (k < 16) { yv[r] = __builtin_fmaf(yu[r], dv, yp[r >> 2][0][r & 3]); KEEP(yv[r]); }
          else if constexpr (k < 40) { yv[r] += yp[r >> 2][(k >> 3) - 1][r & 3]; KEEP(yv[r]); }
          else if constexpr (k < 44) { u32 t = pk(yv[2 * r], yv[2 * r + 1]); KEEP(t); hq[r] = t; }
          else { st128<SC1>(hq, rW, yo); mark<MYS>(); }
        } else if constexpr (j == JB) {
          asm volatile("v_mov_b32 %0, %1" : "=v"(bv[k]) : "v"(bt));
        } else if constexpr (j == JX) {
          if constexpr (k < 4) { u32 t; asm volatile("v_mov_b32 %0, %1" : "=v"(t) : "s"(tag[k == 3])); eo[k == 3 ? 2 : k][k == 3 ? 2 : 3] = t; }
          else {
            constexpr int n = (k - 4) / 11, q = (k - 4) % 11, r = q >> 2, d = q < 8 ? q : 4 * (q - 8) + 3;
            if constexpr (jcost(j, k) == 9) st128<SC1>(eo[r], rW, xo + n * XS + r * 1024);
            else { u32 t = pk(pa[n][2 * d], pa[n][2 * d + 1]); KEEP(t); eo[r][q & 3] = t; }
          }
        } else if constexpr (j == JH) {
          constexpr int n = k / 13, q = k % 13;
          if constexpr (q < 8) { hv[q] = own[0][8 * n + q] * zf[8 * n + q]; KEEP(hv[q]); }
          else if constexpr (q < 12) { u32 t = pk(hv[2 * q - 16], hv[2 * q - 15]); KEEP(t); hq[q - 8] = t; }
          else st128<SC1>(hq, rW, ho + n * 1024);
        } else {
          constexpr int d = k >> 1;
          if constexpr (k & 1) {
            u32 t;
            asm volatile("v_cmp_eq_u32 vcc, %2, %3\n\tv_cndmask_b32 %0, 0, %1, vcc" : "=v"(t) : "v"(it1), "i"((d & 1) * 2 + (d & 6) * 4), "v"(it0) : "vcc");
            idm[d >> 2][d & 3] = t;
          }
        }
        SB();
      };
      auto xadd = [&](f16v& d) AI {
        mv(d, idm[0], pp[0]); mv(d, idm[1], pp[1]);
      };
      it0 = ((l16 >> 4) ^ (l16 >> 7 & 4)) & 30; it1 = 0x3f80u << (l16 & 16);
      KEEP(it0, it1);
      SB();
      sfor<0, 256>([&](auto ii) AI {
        constexpr int i = decltype(ii)::v, s = i >> 4, g = i & 15, vk = PL.vm[i] >> 8, vi = PL.vm[i] & 255, hd_ = PL.head[i] ? 256 : 0;
        if constexpr (i == 128) {
#pragma unroll
          for (int r = 0; r < 16; ++r) own[1][r] = bv[r];
        }
        if constexpr (i < 2) mv0a(pa[i], Zf[i], Wf[0]);
        else if constexpr (i < 128) mva(pa[i & 1], Zf[i], Wf[i >> 1]);
        else if constexpr (i == 128) mvca(own[0], Zf[i], Wf[0], own[1]);
        else mva(own[(i >> 6) & 1], Zf[i], Wf[i & 63]);
        if constexpr (i == E1 + 3 || i == E3 + 3) xadd(own[(i >> 6) & 1]);
        if constexpr (i + RA < 256) rdZ(IC<i + RA>{});
        SB();
        if constexpr (vk == VZ) dmaZ(IC<hd_ + vi>{});
        if constexpr (vk == VS) dmaS(IC<hd_ + vi>{});
        if constexpr (vk == VO) dmaO(IC<hd_ + vi>{});
        if constexpr (vk == VE) { ldE(IC<vi>{}); if constexpr (vi % 3 == 2) mark<MEX + vi / 3>(); }
        if constexpr (vk == VH) ldH(IC<vi>{});
        if constexpr (vk == VF2) { as64<0>(f2v, dW, f2o, 0); mark<MF2>(); }
        if constexpr (vk == VX) { as64<0>(f2v, dW, f3o, 0); mark<MF4>(); }
        if constexpr (vk == VFY) { as64<0>(fy, dW, fyo, 0); mark<MFY>(); }
        SB();
        if constexpr (s + 1 < NCHK) {
          if constexpr (g == 1) zchk(IC<s + 1>{}, IC<0>{});
          if constexpr (g >= 4 && g < 7) zchk(IC<s + 1>{}, IC<g - 3>{});
        }
        if constexpr (s < 15 && g == 7) {
          if constexpr (s == 6) { vw<MYS>(); vneed<MZ + 7>(); } else if constexpr (s + 1 >= NCHK) vw<MZ + s + 1>();
          if constexpr (s == 1) lneed<30>();
          if constexpr (s == 12) vneed<MO + 6>();
          barv();
        }
        if constexpr (RE + 4 * s < 64) { if constexpr (g == 9) sfor<RE + 4 * s, RE + 4 * s + 2>(rdS); if constexpr (g == 10) sfor<RE + 4 * s + 2, RE + 4 * s + 4>(rdS); }
        if constexpr (i < 16 && (i & 1)) { constexpr int n = i >> 1; lw128(cw + (n >> 2) * ZN + (n & 3) * 1024, __builtin_bit_cast(u4, cacc[n])); if constexpr (n == 7) lmark<30>(); }
        if constexpr (i == F2C) { vw<MF2>(); KEEP(f2v); bad2 = fbad(f2v, tag); }
        if constexpr (i == F2C + 2) wait_flags(bad2, dW, f2o, 0, tag);
        if constexpr (HCM && i == F3C) { vw<MF4>(); KEEP(f2v); bad2 = fbad(f2v, tag); }
        if constexpr (HCM && i == F3C + 1) wait_flags(bad2, dW, f3o, 0, tag);
        if constexpr (i >= 24 && i < 32) { constexpr int n = i - 24; yp[n & 1][n >> 1] = __builtin_bit_cast(f4, lr128(ry[n & 1] + (n >> 1) * 4096)); }
        if constexpr (i == 105) st64<SC1>(tag, rW, fa, WS_FY + ch * 128);
        if constexpr (i == E1 || i == E3) { vw<MEX + (i == E3)>(); KEEP(pp[0], pp[1], pp[2]); bad = ebad(); }
        if constexpr (i == E1 + 1 || i == E3 + 1) efix(IC<i == E3 + 1>{});
        if constexpr (i == E1 + 2 || i == E3 + 2) { pp[0][3] = pp[2][0]; pp[1][3] = pp[2][1]; KEEP(pp[0], pp[1]); }
        if constexpr (aidx(i) >= 0) rdO(IC<aidx(i)>{});
        SB();
        sfor<0, NJ>([&](auto jj) AI { sfor<PL.at[decltype(jj)::v][i], PL.at[decltype(jj)::v][i + 1]>([&](auto kk) AI { job(jj, kk); }); });
        if constexpr ((i & 3) == 3 && i + 4 < 256) KEEP(Zf[i + 4]);
        SB();
        if constexpr ((i & 15) == 0 && i) STAMP(24 + s + (s >= 12) * 8);
      });
      nop<7>(); nop<6>();
      SB();
#pragma unroll
      for (int q = 0; q < 2; ++q) {
        u4 o;
#pragma unroll
        for (int d = 0; d < 4; ++d) {
          const int r = 4 * q + d;
          f2 t = (f2){own[1][2 * r], own[1][2 * r + 1]} * (f2){zf[16 + 2 * r], zf[17 + 2 * r]};
          o[d] = pk(t[0], t[1]);
        }
        st128<SC1>(o, rW, ho + 28672 + q * 1024);
        SB();
      }
    }
    STAMP(10);
    {
      auto hchk = [&](auto ii, auto dd) AI {
        constexpr int k = decltype(ii)::v, n = k / 7;
        if constexpr (decltype(dd)::v == 0) {
          vw<MH0 + k>();
          KEEP(Hf[2 * k], Hf[2 * k + 1]);
          bad = bal(max(Hf[2 * k][0], Hf[2 * k + 1][0]) == SENT);
        } else if (__builtin_expect(bad != 0, 0)) {
          COLD();
          vmw<0>();
#pragma nounroll
          for (u32 it = 0; it < SPIN; ++it) {
            sfor<2 * k, 14 * n + 14>([&](auto kk) AI {
              constexpr int m = decltype(kk)::v % 14;
              as128<(m & 3) * 1024>(Hf[14 * n + m], dW, l16, hb + n * 28672 + (m & 12) * 1024);
            });
            vmw<0>();
            u32 mx = 0;
#pragma unroll
            for (int q = k; q < 7 * n + 7; ++q) { KEEP(Hf[2 * q], Hf[2 * q + 1]); mx = mx3(mx, Hf[2 * q][0], Hf[2 * q + 1][0]); }
            if (!anyl(mx == SENT)) break;
          }
        }
      };
      f16v o[2];
      f2 sv[8], xu[8];
      u2 yy[4];
      u32 yb, yt;
      u64 bad2;
      auto ldY = [&](auto nn) AI { as128<0>(yr[decltype(nn)::v], dW, ya, ys + decltype(nn)::v * 65536); mark<MXY>(); };
      auto getY = [&]() AI {
        vw<MXY>();
        KEEP(yr[0], yr[1]);
        if (__builtin_expect(bal(max(mx3(yr[0][0], yr[0][2], yr[1][0]), yr[1][2]) == SENT) != 0, 0)) {
          COLD();
          vmw<0>();
#pragma nounroll
          for (u32 it = 0; it < SPIN; ++it) {
            as128<0>(yr[0], dW, ya, ys); as128<0>(yr[1], dW, ya, ys + 65536);
            vmw<0>();
            KEEP(yr[0], yr[1]);
            if (!anyl(max(mx3(yr[0][0], yr[0][2], yr[1][0]), yr[1][2]) == SENT)) break;
          }
        }
#pragma unroll
        for (int n = 0; n < 2; ++n) lw128(yb + n * 1024, yr[n]);
        FENCE();
#pragma unroll
        for (int r = 0; r < 4; ++r) yy[r] = ltr(yt + r * 512);
        SB();
      };
      auto useY = [&]() AI {
#pragma unroll
        for (int r = 0; r < 8; ++r) { sv[r] *= up(yy[r >> 1][r & 1]); KEEP(sv[r]); }
      };
      sfor<HN, 14>(ldH);
      hchk(IC<0>{}, IC<0>{}); hchk(IC<0>{}, IC<1>{});
      sfor<0, 28>([&](auto ii) AI {
        constexpr int i = decltype(ii)::v;
        if constexpr (i == 0) mx0(o[1], Af[i], Hf[0]);
        else if constexpr (i == 1) mw0(o[0], Af[i], Hf[0]);
        else if constexpr (i & 1) mw(o[0], Af[i], Hf[i >> 1]);
        else mx(o[1], Af[i], Hf[i >> 1]);
        SB();
        if constexpr (!YLATE && (i == YAT + 2 || i == YAT + 4)) ldY(IC<(i - YAT - 2) / 2>{});
        if constexpr (i >= H1AT && i < H1AT + 28 && ((i - H1AT) & 1) == 0) ldH(IC<14 + (i - H1AT) / 2>{});
        if constexpr (i < 24 && (i & 3) >= 2) hchk(IC<i / 4 + 1>{}, IC<i & 1>{});
        if constexpr (i == YAT) { vw<MFY>(); KEEP(fy); bad2 = fbad(fy, tag); }
        if constexpr (i == YAT + 1) wait_flags(bad2, dW, fyo, 0, tag);
        if constexpr (i == 0) { vneed<MO + 13>(); barv(); }
        else rdO(IC<27 + i>{});
        SB();
      });
      rdO(IC<55>{});
      sfor<14 + (29 - H1AT) / 2, 28>(ldH);
      if constexpr (YLATE) sfor<0, 2>(ldY);
      STAMP(12);
      u32 ew, er;
      {
        LANE();
        yb = 8 * ZN + lw4;
        yt = yb + (4 * hh + ((l >> 2) & 3)) * 64 + (g4 & 1) * 32 + (l & 3) * 8;
        ew = l16 + (5 * ZN + (w ^ 1) * 4096); er = l16 + 5 * ZN + lw4;
        const float rsd = asf(ms[1]), mo = -asf(ms[0]) * rsd;
        yb += l16;
        KEEP(xv[0], xv[1], xv[2], xv[3], yb, yt, ew, er);
#pragma unroll
        for (int r = 0; r < 8; ++r) { xu[r] = up(xv[r >> 1][r & 1]); sv[r] = xu[r] * rsd + mo; KEEP(xu[r], sv[r]); }
        if constexpr (!YLATE) { getY(); useY(); }
      }
      STAMP(13);
      hchk(IC<7>{}, IC<0>{}); hchk(IC<7>{}, IC<1>{});
      sfor<0, 28>([&](auto ii) AI {
        constexpr int i = decltype(ii)::v;
        if constexpr (!(i & 1)) mx(o[1], Af[28 + i], Hf[14 + (i >> 1)]);
        else if constexpr (i >= 2 * DL) mw(o[0], Af[28 + i - 2 * DL], Hf[14 + (i >> 1) - DL]);
        SB();
        if constexpr (i < 24 && (i & 3) >= 2) hchk(IC<i / 4 + 8>{}, IC<i & 1>{});
        SB();
      });
      STAMP(14);
      sfor<0, DL>([&](auto ii) AI {
        constexpr int m = 14 - DL + decltype(ii)::v;
        mw(o[0], Af[29 + 2 * m], Hf[14 + m]);
        SB();
        if constexpr (decltype(ii)::v == 0) {
#pragma unroll
          for (int k = 0; k < 4; ++k) lw128(ew + k * 1024, q4(o[1], k));
        }
        SB();
      });
      if constexpr (YLATE) { getY(); useY(); }
      bar();
      f4 pv[4];
#pragma unroll
      for (int n = 0; n < 4; ++n) pv[n] = __builtin_bit_cast(f4, lr128(er + n * 1024));
      SB();
      nop<7>();
      SB();
      const rsrc_t rOut = mk(sub4<3>(kc_));
#pragma unroll
      for (int r = 0; r < 4; ++r) {
        u2 ov;
        const f4 a = pv[r] + (f4){o[0][4 * r], o[0][4 * r + 1], o[0][4 * r + 2], o[0][4 * r + 3]};
#pragma unroll
        for (int d = 0; d < 2; ++d) {
          f2 v = {a[2 * d], a[2 * d + 1]};
          f2 t = v * -1.442695041f;
          f2 e = {__builtin_amdgcn_exp2f(t[0]), __builtin_amdgcn_exp2f(t[1])};
          e = e + 1.f;
          f2 q = {__builtin_amdgcn_rcpf(e[0]), __builtin_amdgcn_rcpf(e[1])};
          v = v * sv[2 * r + d] * q + xu[2 * r + d];
          ov[d] = pk(v[0], v[1]);
        }
        st64<0>(ov, rOut, xa + r * 16, xs);
      }
    }
    STAMPOUT();
  } else {
    asm volatile("; @S");
    constexpr int SOFF = 100; (void)SOFF;
    ENTRY();
    const u32 sp = b - 224;
    u32 l16 = l * 16, pv = (l >> 1) * 4096 + (l & 1) * 128, pfd = 0;
    const u32 cf = 7 * sp + 6 * hh;
    u32 fo = WS_F1 + 16 * ((i32 >> 3) * 1792 + (i32 & 7) * 32 + (cf >> 6) * 256 + ((cf >> 4) & 3) * 8);
    KEEP(l16, pv, fo);
    asm volatile("s_waitcnt lgkmcnt(0)" : "+s"(ka), "+s"(kb), "+s"(kc_));
    STAMP0();
    const u4 dW = sub4<2>(ka);
    const rsrc_t rW = mk(dW);
    u64 e0, d3; u32 d1;
    asm volatile("s_load_dwordx2 %0, %3, 0\n\ts_load_dwordx2 %1, %4, %5\n\ts_load_dword %2, %4, %6" : "=&s"(e0), "=&s"(d3), "=&s"(d1)
                 : "s"(((u64)dW[1] << 32) | dW[0]), "s"(((u64)kc_[1] << 32) | kc_[0]), "s"(((7 * sp) >> 1) * 4), "s"(((7 * sp + 3 + w) >> 1) * 4));
    const u32 ps = (((b & 7) >> 1) * 512 + ((sp >> 3) * 4 + w) * 32) * 4096 + (b & 1) * 2048;
    u4 hd[7]; u2 hp[7];
    auto pfs = [&](auto ii) AI { constexpr int i = decltype(ii)::v; if constexpr (i < PFS) pf(pfd, sub4<3>(ka), pv, ps + i * 256); };
    u4 sent = {SENT, SENT, SENT, SENT};
    u32 zc = l16 + (sp < 28 ? WS_Z + sp * 131072 + (~sp & 2) * 32768 + w * (NCHK * 2048) : OOB), yc = l16 + (WS_Y + (sp * 4 + w) * 7168), hc = l16 + (WS_H + (sp * 4 + w) * 28672), f0 = l >= FL ? OOB : l * 8;
    u32 t16 = tid * 16, t8 = (t16 - 8) | ((tid - 1) & OOB);
    KEEP(sent, zc, yc, hc, f0, t16, t8);
    sfor<0, 2 * NCHK>([&](auto ii) AI { constexpr int i = decltype(ii)::v; st128<SC1>(sent, rW, opq(zc + (i >> 2) * 4096) + (i & 3) * 1024); });
    mark<MK>();
    sfor<0, PFH>(pfs);
    STAMP(3);
    asm volatile("s_waitcnt lgkmcnt(0)" : "+s"(e0), "+s"(d3), "+s"(d1) :: "memory");
    const u2 tag = next_tag(e0);
    vw<MK>();
    st64<SC1>(tag, rW, f0, WS_FZ + (sp * 4 + w) * 128);
    STAMP(7);
    sfor<PFH, PFG>(pfs);
    sfor<0, 7>([&](auto ii) AI {
      constexpr int k = decltype(ii)::v;
      al128<0>(hd[k], sub4<3>(kb), t16, (7 * sp + k) * 4096);
      al64<0>(hp[k], sub4<3>(kb), t8, (7 * sp + k) * 4096);
    });
    mark<MSH>();
    sfor<PFG, PF1>(pfs);
    STAMP(1);
    vw<MSH>();
    barv();
    constexpr u32 CS = 21056, PB = 7 * CS + 64;
#pragma unroll
    for (int k = 0; k < 7; ++k) { KEEP(hd[k], hp[k]); put_copies(opq(k * CS + 32 + t16), hd[k], hp[k]); }
#pragma unroll
    for (int r = 0; r < 2; ++r) lw128(min(w + 4 * r, 6u) * CS + (l < 8 ? (l >> 1) * 4128 + (l & 1) * 16 : 20608 + min(l - 8, 29u) * 16), (u4){0, 0, 0, 0});
    sfor<0, 7>([&](auto ii) AI { constexpr int i = decltype(ii)::v; st128<SC1>(sent, rW, opq(yc + (i >> 2) * 4096) + (i & 3) * 1024); });
    u32 ha[4][2], ua[4], ry[3][2], cw[2], dl, yl, yo, uo;
    float dv[4];
    {
      const u32 hl = (i16 & 3) * 4128 + 8 + 16 * g4 + 8 * (i16 >> 2), ul = 16512 + 480 - 32 * i16 + 16 * g4;
#pragma unroll
      for (int k = 0; k < 4; ++k) {
        const u32 o = k < 3 ? k * CS + 128 * w : (3 + w) * CS;
        ha[k][0] = hl + o; ha[k][1] = ha[k][0] + 2048; ua[k] = ul + o;
        KEEP(ha[k][0], ha[k][1], ua[k]);
        dv[k] = k < 3 ? asf((u32)(d3 >> (16 * (k + (sp & 1)))) << 16) : asf((d1 >> (((sp + 1 + w) & 1) * 16)) << 16);
      }
#pragma unroll
      for (int k = 0; k < 3; ++k) {
        ry[k][0] = ((w < 2 ? k * CS + 64 : PB) + (w & 1) * 2048) + ((2 * l16) ^ ((l16 >> 2) & 48)); ry[k][1] = ry[k][0] ^ 16;
        KEEP(ry[k][0], ry[k][1]);
      }
      cw[0] = 64 + w * 4096 + i16 * 64 + ((g4 ^ ((i16 >> 1) & 3)) << 4); cw[1] = cw[0] + (PB - 64);
      yl = 32 * i16 + 8 * g4; dl = (3 + w) * CS + 16512 + 4088 - 3584 - yl;
      yo = t16 + (WS_Y + sp * 28672); uo = 16512 + 4080 - t16;
      KEEP(cw[0], cw[1], yl, dl, yo, uo);
    }
    STAMP(2);
    vmw<0>();
    STAMP(11);
    if (w == 0) wait_flags(1, dW, fo, 0, tag);
    barv();
    STAMP(4);
    sfor<0, 7>([&](auto ii) AI { constexpr int k = decltype(ii)::v; dmas<k * CS + 16512, k * 4096>(dW, t16, opqs(WS_KV + 7 * sp * 4096), w * 1024); });
    st128<SC1>((u4){tag[0], tag[1], tag[0], tag[1]}, rW, (tid < 56 ? t16 : OOB) + (WS_FY + sp * 896));
    FENCE();
    sfor<PF1, 8>(pfs);
    pf(pfd, sub4<2>(kc_), l < 49 ? l * 128 : OOB, (sp * 4 + w) * 6272);
    vmw<10 - PF1>();
    STAMP(5);
    bar();
    STAMP(6);
    u4 Af[15][8], Bf[15][8], ud, hq;
    f4 acc[4][8], yp[2][4];
    u2 uu[8];
    float yu[8], yv[8], ci[32];
    auto rdf = [&](auto rr, auto ff) AI {
      constexpr int r = decltype(rr)::v, f = decltype(ff)::v, a = f >> 1, k = r < 6 ? r >> 1 : 3, o = 64 * (r < 6 ? r & 1 : r - 6);
      if constexpr (f & 1) Bf[r][a] = lr128(ua[k] + 512 * (7 - a) + o); else Af[r][a] = lr128a8(ha[k][a >> 2] + 512 * (a & 3) + o);
    };
    sfor<0, 16>([&](auto ff) AI { rdf(IC<0>{}, ff); });
    SB();
    sfor<0, 14 * 36>([&](auto ii) AI {
      constexpr int i = decltype(ii)::v, r = i / 36, k = i % 36, c = r < 6 ? r >> 1 : 3, n = cta[k] + ctb[k], j = i % 72, m = i / 72 - 1;
      if constexpr (j == 0) STAMP(20 + i / 72);
      if constexpr (r == 6 && k == 0) {
#pragma unroll
        for (int q = 0; q < 8; ++q) acc[3][q] = (f4){ci[4 * q], ci[4 * q + 1], ci[4 * q + 2], ci[4 * q + 3]};
      }
      if constexpr (r < 6 && !(r & 1) && cfirst(k)) mv160(acc[c][n], Af[r][cta[k]], Bf[r][ctb[k]]);
      else mv16(acc[c][n], Af[r][cta[k]], Bf[r][ctb[k]]);
      if constexpr (r < 13 && k >= 3 && k < 35 && (k & 1)) rdf(IC<r + 1>{}, IC<(k - 3) / 2>{});
      SB();
      if constexpr (HCM && i >= HCK && i < HCK + 28 * HCS && (i - HCK) % HCS == 0) { constexpr int q = (i - HCK) / HCS; st128<SC1>(sent, rW, hc + (q & 3) * 1024); if constexpr ((q & 3) == 3) { hc += 4096; KEEP(hc); } SB(); }
      if constexpr (HCM && i == HCF) { vmw<0>(); st64<SC1>(tag, rW, f0, WS_FH + (sp * 4 + w) * 128); SB(); }
      if constexpr (m < 0) {
        if constexpr (j >= 8 && j < 24 && !(j & 1)) uu[(j - 8) >> 1] = lr64(dl + 512 * (7 - ((j - 8) >> 1)));
        if constexpr (j >= 38 && j < 70) {
          constexpr int q = j - 38;
          const u32 d = uu[q >> 2][1 - (q >> 1 & 1)];
          ci[q] = opf((q & 1) ? blo(d) : bhi(d)) * dv[3];
          KEEP(ci[q]);
        }
      } else if constexpr (m < 3) {
        if constexpr (j == 2) { if constexpr (m) lneed<41>(); barv(); }
        if constexpr (j >= 4 && j < 20 && !(j & 1)) { constexpr int q = (j - 4) >> 1; lw128(cw[q >> 2] + (q < 4 ? m * CS : 0) + (q & 3) * 1024, __builtin_bit_cast(u4, acc[m][q])); if constexpr (q == 7) lmark<40>(); }
        if constexpr (j == 20) ud = lr128(uo + m * CS);
        if constexpr (j == 30) { lgw<6>(); lneed<40>(); barv(); }
        if constexpr (j >= 32 && j < 48 && !(j & 1)) { constexpr int q = (j - 32) >> 1; yp[q & 1][q >> 1] = __builtin_bit_cast(f4, lr128(ry[m][q & 1] + (q >> 1) * 4096)); if constexpr (q == 7) lmark<41>(); }
        if constexpr (j >= 40 && j < 48) { constexpr int q = j - 40; yu[q] = (q & 1) ? blo(ud[3 - (q >> 1)]) : bhi(ud[3 - (q >> 1)]); KEEP(yu[q]); }
        if constexpr (j >= 52 && j < 68) {
          sfor<2 * (j - 52), 2 * (j - 52) + 2>([&](auto qq) AI {
            constexpr int q = decltype(qq)::v & 7, t = decltype(qq)::v >> 3;
            if constexpr (t == 0) yv[q] = __builtin_fmaf(yu[q], dv[m], yp[q >> 2][0][q & 3]); else yv[q] += yp[q >> 2][t][q & 3];
            KEEP(yv[q]);
          });
        }
        if constexpr (j == 68 || j == 69) { u32 t0 = pk(yv[4 * (j - 68)], yv[4 * (j - 68) + 1]), t1 = pk(yv[4 * (j - 68) + 2], yv[4 * (j - 68) + 3]); KEEP(t0, t1); hq[2 * (j - 68)] = t0; hq[2 * (j - 68) + 1] = t1; }
        if constexpr (j == 70) { st128<SC1>(hq, rW, yo); yo += 4096; KEEP(yo); }
      }
      if constexpr (r == 13) {
        sfor<0, 8>([&](auto qq) AI {
          constexpr int q = decltype(qq)::v;
          if constexpr (clast(q) + 3 == k) st64<SC1>((u2){pk(acc[3][q][0], acc[3][q][1]), pk(acc[3][q][2], acc[3][q][3])}, rW, yl + 512 * q, WS_Y + (7 * sp + 3 + w) * 4096);
        });
      }
      SB();
    });
    nop<7>();
    SB();
    sfor<0, 8>([&](auto qq) AI {
      constexpr int q = decltype(qq)::v;
      if constexpr (clast(q) + 3 > 35) st64<SC1>((u2){pk(acc[3][q][0], acc[3][q][1]), pk(acc[3][q][2], acc[3][q][3])}, rW, yl + 512 * q, WS_Y + (7 * sp + 3 + w) * 4096);
    });
    STAMP(12);
    vmw<0>();
    KEEP(pfd);
    if (sp == 31 && w == 3) {
#pragma nounroll
      for (u32 it = 0; it < SPIN; ++it) {
        u32 bad = 0;
#pragma unroll
        for (int n = 0; n < 7; ++n) { u2 f = ld64<SC1>(rW, l * 128, WS_FY + n * 8192); bad |= (f[0] ^ tag[0]) | (f[1] ^ tag[1]); }
        FENCE();
        if (!anyl(bad != 0)) break;
        __builtin_amdgcn_s_sleep(8);
      }
      st64<SC1>(tag, rW, l ? OOB : 0, WS_E);
    }
    STAMPOUT();
  }
}
}

extern "C" void kernel_launch(void* const* d_in, const int* in_sizes, int n_in, void* d_out, int out_size, void* d_ws, size_t ws_size,
                              hipStream_t stream) {
  constexpr unsigned F = 0x00020000u;
  fk::fused<<<256, 256, 0, stream>>>(d_in[0], 2048 * 896, F, d_in[7], 448 * 3584, F, d_ws, fk::WS_END, F, d_in[8], 2048 * 4096, F,
                                     d_in[1], 2048 * 896, F, d_in[2], 2048 * 896, F, d_in[3], 448 * 8, F, d_in[4], 448 * 4096, F,
                                     d_in[5], 896, F, d_in[9], 4096, F, d_in[10], 896 * 896, F, d_out, 2048 * 896, F);
}
```
